```python
import torch

def _softmax(x, axis=-1):
    return torch.softmax(x, dim=axis)
def _gelu(x):
    return torch.nn.functional.gelu(x, approximate="tanh")
def _silu(x):
    return torch.nn.functional.silu(x)
def _sigmoid(x):
    return torch.sigmoid(x)
def _layernorm(x, axis=-1, eps=1e-5):
    m = x.mean(dim=axis, keepdim=True)
    v = x.var(dim=axis, keepdim=True, unbiased=False)
    return (x - m) / torch.sqrt(v + eps)
def _scatter_add(tgt, idx, src, axis=0):
    return tgt.clone().index_add_(axis, idx, src)
def _segment_sum(src, seg, n):
    out = torch.zeros((n,) + src.shape[1:], dtype=src.dtype)
    return out.index_add_(0, seg, src)
def _pad(x, pw):
    flat = tuple(v for pair in reversed(pw) for v in pair)
    return torch.nn.functional.pad(x, flat)

kernel_name = 'recurrent_scan__ssm_conv_s4__mixer_s_mlp__prompt'

def setup_inputs():

    _w = (lambda a: (lambda t: t.to(torch.bfloat16) if t.is_floating_point() else t)(torch.as_tensor(a)))
    def _A_setup():
        B, L, D, N = 1, 2048, 512, 128
        A_re = -(lambda _x: torch.exp(torch.as_tensor(_x)))((lambda *s: torch.randn(*s, dtype=torch.bfloat16) * 3.2)(D, N))
        A_im = torch.pi * (lambda *s: torch.randn(*s, dtype=torch.bfloat16) * 0.05)(D, N)
        B_re = (lambda *s: torch.randn(*s, dtype=torch.bfloat16) * 0.1)(D, N)
        B_im = (lambda *s: torch.randn(*s, dtype=torch.bfloat16) * 0.1)(D, N)
        C_re = (lambda *s: torch.randn(*s, dtype=torch.bfloat16) * 0.1)(D, N)
        C_im = (lambda *s: torch.randn(*s, dtype=torch.bfloat16) * 0.1)(D, N)
        log_dt = (lambda *s: torch.randn(*s, dtype=torch.bfloat16) * 0.05)(D) * 0.5 - 4.0
        Dskip = (lambda *s: torch.randn(*s, dtype=torch.bfloat16) * 0.05)(D)
        return {
            "u": _w((lambda *s: torch.randn(*s, dtype=torch.bfloat16) * 0.05)(B, L, D)),
            "A_re": _w(A_re),
            "A_im": _w(A_im),
            "B_re": _w(B_re),
            "B_im": _w(B_im),
            "C_re": _w(C_re),
            "C_im": _w(C_im),
            "log_dt": _w(log_dt),
            "Dskip": _w(Dskip),
        }
    _A_in = _A_setup()

    def _B_setup():
        B, S, D, F = 1, 2048, 512, 1376
        return {
            "x":  _w((lambda *s: torch.randn(*s, dtype=torch.bfloat16) * 0.05)(B, S, D)),
            "w1": _w((lambda *s: torch.randn(*s, dtype=torch.bfloat16) * 3.2)(D, D) * (D ** -0.5)),
            "w2": _w((lambda *s: torch.randn(*s, dtype=torch.bfloat16) * 3.2)(D, D) * (D ** -0.5)),
            "w3": _w((lambda *s: torch.randn(*s, dtype=torch.bfloat16) * 3.2)(D, F) * (D ** -0.5)),
            "w4": _w((lambda *s: torch.randn(*s, dtype=torch.bfloat16) * 1.6)(F, D) * (F ** -0.5)),
        }
    _B_in = _B_setup()

    return {
        "A_u": _A_in["u"],
        "A_A_re": _A_in["A_re"],
        "A_A_im": _A_in["A_im"],
        "A_B_re": _A_in["B_re"],
        "A_B_im": _A_in["B_im"],
        "A_C_re": _A_in["C_re"],
        "A_C_im": _A_in["C_im"],
        "A_log_dt": _A_in["log_dt"],
        "A_Dskip": _A_in["Dskip"],
        "B_x": _B_in["x"],
        "B_w1": _B_in["w1"],
        "B_w2": _B_in["w2"],
        "B_w3": _B_in["w3"],
        "B_w4": _B_in["w4"],
    }

def reference(A_u, A_A_re, A_A_im, A_B_re, A_B_im, A_C_re, A_C_im, A_log_dt, A_Dskip, B_x, B_w1, B_w2, B_w3, B_w4):
    def _A_ref(u, A_re, A_im, B_re, B_im, C_re, C_im, log_dt, Dskip):
        Bsz, L, D = u.shape
        dt = torch.exp(log_dt)[:, None]
        A = A_re + 1j * A_im
        Bc = B_re + 1j * B_im
        Cc = C_re + 1j * C_im
        logdA = dt * A
        dB = dt * Bc
        l = (lambda x, dt: x.to(dt))(torch.arange(L), A_re.dtype)
        P = torch.exp(logdA[:, :, None] * l[None, None, :])
        K = torch.einsum("dn,dnl,dn->dl", Cc, P, dB).real
        L2 = 2 * L
        Kp = _pad(K, ((0, 0), (0, L)))
        up = (lambda x, *d: x.permute(*d))(u, 0, 2, 1)
        up = _pad(up, ((0, 0), (0, 0), (0, L)))
        Kf = (lambda x, axes=(-2,-1): torch.fft.rfft2(x.float(), dim=axes))(Kp[:, None, :], axes=(-2, -1))
        Uf = (lambda x, axes=(-2,-1): torch.fft.rfft2(x.float(), dim=axes))(up[:, :, None, :], axes=(-2, -1))
        Yf = Uf * Kf[None, :, :, :]
        y = (lambda x, s, axes=(-2,-1): torch.fft.irfft2(x, s=s, dim=axes).to(torch.bfloat16))(Yf, (1, L2), axes=(-2, -1))[:, :, 0, :L]
        y = (lambda x, *d: x.permute(*d))(y, 0, 2, 1) + u * Dskip[None, None, :]
        return y

    def _B_ref(x, w1, w2, w3, w4):
        B, S, D = x.shape
        h = _gelu(_layernorm(x, axis=-1) @ w1)
        c = D // 4
        g0 = _pad(h[:, :, 0*c:1*c], ((0, 0), (1, 0), (0, 0)))[:, :S, :]
        g1 = _pad(h[:, :, 1*c:2*c], ((0, 0), (0, 1), (0, 0)))[:, 1:, :]
        g2 = _pad(h[:, :, 2*c:3*c], ((0, 0), (2, 0), (0, 0)))[:, :S, :]
        g3 = _pad(h[:, :, 3*c:   ], ((0, 0), (0, 2), (0, 0)))[:, 2:, :]
        hs = torch.concatenate([g0, g1, g2, g3], axis=-1) @ w2
        x1 = x + hs
        h2 = _gelu(_layernorm(x1, axis=-1) @ w3) @ w4
        return x1 + h2

    x = A_u
    xn = _layernorm(x, axis=-1)
    s = _A_ref(xn, A_A_re, A_A_im, A_B_re, A_B_im, A_C_re, A_C_im, A_log_dt, A_Dskip)
    g = _silu(_B_ref(xn, B_w1, B_w2, B_w3, B_w4))
    return x + s * g
```

```cpp
#include <hip/hip_runtime.h>
#include <hip/hip_bf16.h>

struct Desc { const void* p; unsigned bytes; unsigned word3; };
struct Args { Desc d[7]; const void* par[8]; unsigned long long fill[2]; };

__global__ __launch_bounds__(256, 1) void fused_s4_mixer_kernel(Args a) {
  asm volatile(
    ".Lstart_%=:\n"
    "s_getpc_b64 s[4:5]\n"
    "s_load_dwordx16 s[16:31], %0, 0x0\n"
    "s_load_dwordx16 s[32:47], %0, 0x40\n"
    "s_load_dwordx16 s[48:63], %0, 0x80\n"
    "v_and_b32 v255, 63, %2\n"
    "v_lshrrev_b32 v2, 6, %2\n"
    "s_lshr_b32 s64, %1, 3\n"
    "v_readfirstlane_b32 s3, v2\n"
    "s_and_b32 s12, %1, 7\n"
    "s_and_b32 s6, s64, 7\n"
    "s_lshr_b32 s7, s64, 3\n"
    "s_lshl2_add_u32 s8, s12, s7\n"
    "s_lshl2_add_u32 s64, s64, s3\n"
    "s_lshl_b32 s64, s64, 9\n"
    "v_and_b32 v2, 3, v255\n"
    "v_lshl_add_u32 v2, v2, 7, s64\n"
    "s_mov_b32 s65, .Lend_%=-.Lstart_%=-4\n"
    "v_min_u32 v2, s65, v2\n"
    "v_and_b32 v253, 3, v255\n"
    "v_lshlrev_b32 v253, 7, v253\n"
    "v_add_u32 v253, s64, v253\n"
    "v_lshlrev_b32 v253, 3, v253\n"
    "s_lshr_b32 s9, s6, 2\n"
    "s_add_u32 s9, s9, 1\n"
    "global_load_dword v254, v2, s[4:5]\n"
    "s_sub_u32 s64, 0, s9\n"
    "s_bitcmp1_b32 s6, 1\n"
    "s_cselect_b32 s9, s9, s64\n"
    "s_and_b32 s10, s3, 1\n"
    "s_lshr_b32 s11, s3, 1\n"
    "v_lshlrev_b32 v252, 4, v255\n"
    "v_lshrrev_b32 v251, 4, v255\n"
    "v_lshrrev_b32 v250, 3, v255\n"
    "s_lshl_b32 s13, s8, 6\n"
    "s_add_u32 s13, s13, s9\n"
    "s_lshl4_add_u32 s64, s3, s13\n"
    "v_and_b32 v2, 7, v255\n"
    "v_xor_b32 v2, v2, v251\n"
    "v_lshlrev_b32 v2, 4, v2\n"
    "v_add_u32 v3, s64, v250\n"
    "v_lshl_add_u32 v248, v3, 10, v2\n"
    "v_xor_b32 v249, 64, v248\n"
    "v_add_u32 v249, 0x2000, v249\n"
    "s_cmp_lt_i32 s9, 0\n"
    "s_cselect_b32 s14, 64, -2\n"
    "s_add_u32 s14, s14, s10\n"
    "s_add_u32 s66, s14, s13\n"
    "s_lshl_b32 s66, s66, 10\n"
    "v_and_b32 v3, 1, v251\n"
    "v_lshrrev_b32 v4, 5, v255\n"
    "v_lshl_or_b32 v3, v4, 1, v3\n"
    "v_bfe_u32 v4, v255, 1, 2\n"
    "v_xor_b32 v3, v3, v4\n"
    "v_and_b32 v4, 1, v255\n"
    "v_lshlrev_b32 v4, 4, v4\n"
    "v_lshl_or_b32 v247, v3, 5, v4\n"
    "s_lshl_b32 s64, s6, 7\n"
    "v_lshl_add_u32 v2, v250, 10, s64\n"
    "v_add_u32 v10, v2, v247\n"
    "v_xor_b32 v11, 64, v10\n"
    "v_add_u32 v11, 0x1c00, v11\n"
    "v_add_u32 v12, 0x3800, v10\n"
    "v_add_u32 v13, 0x3800, v11\n"
    "s_lshl_b32 s15, s3, 14\n"
    "s_add_u32 s15, s15, 0x10800\n"
    "v_lshlrev_b32 v246, 1, v255\n"
    "s_lshl2_add_u32 s4, %1, s3\n"
    "s_and_b32 s4, s4, 0xff\n"
    "s_lshl_b32 s4, s4, 7\n"
    "s_add_u32 s4, s4, 0x1000\n"
    "v_min_u32 v245, 1, v255\n"
    "v_mul_u32_u24 v245, s4, v245\n"
    "v_mov_b32 v22, 0x3f803f80\n"
    "v_mov_b32 v23, 0x3f803f80\n"
    "v_mov_b32 v24, 0x3f803f80\n"
    "v_mov_b32 v25, 0x3f803f80\n"
    "s_mov_b32 s76, 0x3b000000\n"
    "s_mov_b32 s77, 0x3727c5ac\n"
    "s_mov_b32 s78, 0xffff0000\n"
    "v_and_b32 v244, 15, v255\n"
    "v_lshrrev_b32 v26, 1, v244\n"
    "v_xor_b32 v26, v26, v251\n"
    "v_lshlrev_b32 v26, 4, v26\n"
    "v_lshl_add_u32 v26, v244, 7, v26\n"
    "v_add_u32 v14, s15, v26\n"
    "v_xor_b32 v27, 64, v26\n"
    "v_add_u32 v15, s15, v27\n"
    "s_xor_b32 s4, s3, 1\n"
    "s_lshl_b32 s4, s4, 14\n"
    "s_add_u32 s4, s4, 0x10800\n"
    "v_add_u32 v16, s4, v26\n"
    "v_add_u32 v17, s4, v27\n"
    "v_bfe_u32 v243, v255, 2, 2\n"
    "v_and_b32 v242, 3, v255\n"
    "v_lshlrev_b32 v242, 3, v242\n"
    "v_lshrrev_b32 v26, 1, v243\n"
    "v_and_b32 v27, 1, v251\n"
    "v_lshl_or_b32 v27, v27, 1, v26\n"
    "s_lshl1_add_u32 s4, s10, s11\n"
    "v_xor_b32 v27, s4, v27\n"
    "v_lshl_add_u32 v28, v251, 3, v243\n"
    "v_lshl_add_u32 v28, v28, 7, v242\n"
    "v_lshl_add_u32 v18, v27, 5, v28\n"
    "v_xor_b32 v19, 32, v18\n"
    "v_xor_b32 v20, 64, v18\n"
    "v_xor_b32 v21, 0x60, v18\n"
    "v_lshrrev_b32 v27, 1, v251\n"
    "v_xor_b32 v27, v27, v251\n"
    "v_and_b32 v27, 1, v27\n"
    "v_lshl_or_b32 v234, v27, 1, v26\n"
    "v_lshl_add_u32 v27, v251, 2, v243\n"
    "v_lshl_add_u32 v27, v27, 7, v242\n"
    "v_lshl_add_u32 v235, v234, 5, v27\n"
    "v_and_b32 v26, 1, v255\n"
    "v_cmp_eq_u32 s[72:73], 1, v26\n"
    "v_and_b32 v26, 2, v255\n"
    "v_cmp_eq_u32 s[74:75], 2, v26\n"
    "v_lshrrev_b32 v26, 2, v244\n"
    "v_cmp_eq_u32 vcc, v26, v251\n"
    "s_lshl_b32 s4, s3, 8\n"
    "s_add_u32 s5, s4, 0x20800\n"
    "v_lshl_add_u32 v238, v244, 4, s5\n"
    "v_mov_b32 v27, 0x20f40\n"
    "v_cndmask_b32 v241, v27, v238, vcc\n"
    "s_lshl4_add_u32 s5, s3, s9\n"
    "s_lshl_b32 s5, s5, 2\n"
    "s_add_u32 s5, s5, 0x20c10\n"
    "v_lshl_add_u32 v26, v244, 2, s5\n"
    "v_cndmask_b32 v240, v27, v26, vcc\n"
    "s_xor_b32 s4, s4, 0x100\n"
    "s_add_u32 s4, s4, 0x20800\n"
    "v_lshl_add_u32 v239, v244, 4, s4\n"
    "s_lshl_b32 s4, s3, 6\n"
    "s_add_u32 s5, s4, 0x20e40\n"
    "v_lshl_add_u32 v236, v251, 4, s5\n"
    "s_xor_b32 s4, s4, 0x80\n"
    "s_add_u32 s5, s4, 0x20e40\n"
    "v_lshl_add_u32 v237, v251, 4, s5\n"
    "s_lshl_b32 s79, s6, 5\n"
    "s_add_u32 s79, s79, s8\n"
    "s_lshl1_add_u32 s79, s79, s11\n"
    "s_lshl1_add_u32 s64, s79, s10\n"
    "s_lshl_b32 s64, s64, 7\n"
    "v_add_u32 v246, s64, v246\n"
    "s_lshl_b32 s65, s79, 1\n"
    "v_mov_b32 v233, s65\n"
    "s_lshl_b32 s80, s3, 12\n"
    "s_waitcnt lgkmcnt(0)\n"
    "s_sub_u32 s64, 0x180, s20\n"
    "s_and_b32 s64, s64, 0x380\n"
    "buffer_load_dword v254, v253, s[20:23], s64 offen\n"
    "s_bfe_u32 s64, s20, 0x30007\n"
    "s_add_u32 s64, s64, s6\n"
    "s_and_b32 s64, s64, 7\n"
    "s_cmp_eq_u32 s64, 3\n"
    "s_cselect_b32 s64, s7, 0\n"
    "s_add_u32 s64, s64, s12\n"
    "s_lshr_b32 s71, s6, 1\n"
    "s_add_u32 s64, s64, s71\n"
    "s_and_b32 s68, s64, 3\n"
    "s_add_u32 s69, s64, 1\n"
    "s_and_b32 s69, s69, 3\n"
    "s_add_u32 s70, s64, 2\n"
    "s_and_b32 s70, s70, 3\n"
    "s_add_u32 s71, s64, 3\n"
    "s_and_b32 s71, s71, 3\n"
    "s_lshl_b32 s64, s68, 8\n"
    "s_add_u32 m0, s15, 0\n"
    "s_sub_u32 s28, s28, 0x800\n"
    "buffer_load_dwordx4 v248, s[16:19], s64 offen lds\n"
    "s_subb_u32 s29, s29, 0\n"
    "s_add_u32 m0, s15, 0x400\n"
    "s_add_u32 s30, s30, 0x800\n"
    "s_lshl2_add_u32 s4, %1, s3\n"
    "s_bfe_u32 s4, s20, 0x30007\n"
    "s_add_u32 s4, s4, s6\n"
    "s_add_u32 s4, s4, 4\n"
    "s_and_b32 s4, s4, 7\n"
    "s_mul_i32 s5, s8, 7\n"
    "s_add_u32 s5, s5, s4\n"
    "s_lshl2_add_u32 s5, s5, s3\n"
    "s_cmp_eq_u32 s4, 7\n"
    "s_cselect_b32 s4, 0x10000, s5\n"
    "s_mul_i32 s5, s4, 0x280\n"
    "buffer_load_dwordx4 v249, s[16:19], s64 offen lds\n"
    "s_add_u32 m0, s15, 0x780\n"
    "s_mul_i32 s4, s4, 0x680\n"
    "v_min_u32 v160, 4, v255\n"
    "v_min_u32 v161, 12, v255\n"
    "v_lshl_add_u32 v161, v161, 7, s4\n"
    "v_lshl_add_u32 v160, v160, 7, s5\n"
    "v_mov_b32 v30, -1\n"
    "v_mov_b32 v31, -1\n"
    "v_mov_b32 v32, -1\n"
    "v_mov_b32 v33, -1\n"
    "v_lshlrev_b32 v232, 3, v255\n"
    "s_mul_i32 s4, s8, 0x2b000\n"
    "s_add_u32 s83, s4, 0x840000\n"
    "s_lshl2_add_u32 s4, s6, s3\n"
    "buffer_load_dwordx4 v248, s[16:19], s64 offen offset:128 lds\n"
    "s_add_u32 m0, s15, 0xb80\n"
    "s_lshl_b32 s4, s4, 10\n"
    "s_add_u32 s82, s83, s4\n"
    "s_lshl1_add_u32 s4, s6, s11\n"
    "s_add_u32 s5, s6, 3\n"
    "s_cmp_lt_u32 s6, 3\n"
    "s_cselect_b32 s4, s4, s5\n"
    "s_lshl1_add_u32 s4, s4, s10\n"
    "s_lshl_b32 s4, s4, 9\n"
    "s_add_u32 s4, s4, 0x8000\n"
    "s_add_u32 s83, s83, s4\n"
    "s_cmp_ge_u32 s6, 3\n"
    "s_cselect_b32 s4, s11, 0\n"
    "s_cmp_lg_u32 s4, 0\n"
    "buffer_load_dwordx4 v249, s[16:19], s64 offen offset:128 lds\n"
    "s_cselect_b32 s83, 0x7f000000, s83\n"
    "s_lshl2_add_u32 s64, s68, s3\n"
    "s_lshl_b32 s64, s64, 15\n"
    "s_add_u32 m0, s80, 0\n"
    "v_lshlrev_b32 v229, 3, v244\n"
    "v_and_b32 v228, 0xf8, v232\n"
    "s_lshl2_add_u32 s60, s8, s3\n"
    "s_lshl_b32 s60, s60, 14\n"
    "s_lshl_b32 s5, s6, 11\n"
    "s_add_u32 s60, s60, s5\n"
    "s_add_u32 s61, s60, 0x240000\n"
    "s_lshl3_add_u32 s87, s8, s6\n"
    "s_lshl2_add_u32 s62, s8, s3\n"
    "s_lshl3_add_u32 s62, s62, s6\n"
    "buffer_load_dwordx4 v10, s[20:23], s64 offen lds\n"
    "s_lshl_b32 s62, s62, 7\n"
    "s_add_u32 s62, s62, 0xdc0000\n"
    "s_lshl_b32 s4, s11, 11\n"
    "s_add_u32 s5, s4, 0x10800\n"
    "s_xor_b32 s4, s4, 0x800\n"
    "s_add_u32 s4, s4, 0x10800\n"
    "v_add_u32 v46, s5, v235\n"
    "v_add_u32 v50, s4, v235\n"
    "v_xor_b32 v2, 32, v235\n"
    "v_add_u32 v47, s5, v2\n"
    "v_add_u32 v51, s4, v2\n"
    "v_xor_b32 v2, 64, v235\n"
    "buffer_load_dwordx4 v11, s[20:23], s64 offen offset:1024 lds\n"
    "v_add_u32 v48, s5, v2\n"
    "v_add_u32 v52, s4, v2\n"
    "v_xor_b32 v2, 0x60, v235\n"
    "v_add_u32 v49, s5, v2\n"
    "v_add_u32 v53, s4, v2\n"
    "v_min_u32 v227, 1, v255\n"
    "v_lshlrev_b32 v227, 30, v227\n"
    "s_min_u32 s4, s6, 3\n"
    "s_lshl_b32 s4, s4, 5\n"
    "s_mul_i32 s86, s6, 0xa0\n"
    "s_add_u32 s86, s86, s4\n"
    "s_lshl_b32 s4, s10, 6\n"
    "buffer_load_dwordx4 v12, s[20:23], s64 offen offset:2048 lds\n"
    "v_xor_b32 v3, s4, v247\n"
    "v_mul_u32_u24 v2, 0xac0, v250\n"
    "v_add_u32 v2, v2, v3\n"
    "s_bfe_u32 s90, s86, 0x10005\n"
    "v_xor_b32 v4, s90, v250\n"
    "v_and_b32 v4, 1, v4\n"
    "v_lshlrev_b32 v4, 6, v4\n"
    "v_add_u32 v2, v2, v4\n"
    "v_add_u32 v224, 0x800, v2\n"
    "v_add_u32 v225, 0x480, v2\n"
    "v_and_b32 v5, v4, v3\n"
    "v_mul_u32_u24 v5, 6, v5\n"
    "buffer_load_dwordx4 v13, s[20:23], s64 offen offset:3072 lds\n"
    "v_sub_u32 v2, v2, v5\n"
    "v_add_u32 v226, 0x100, v2\n"
    "v_xor_b32 v3, v3, v4\n"
    "s_cmp_ge_u32 s6, 3\n"
    "s_cselect_b32 s4, 64, 0x7f000000\n"
    "v_cmp_le_u32 vcc, s4, v3\n"
    "v_mov_b32 v4, 0x7f000000\n"
    "s_nop 0\n"
    "v_cndmask_b32 v226, v226, v4, vcc\n"
    "s_mul_i32 s4, s3, 0x5600\n"
    "s_lshl1_add_u32 s4, s86, s4\n"
    "s_and_b32 s5, s12, 3\n"
    "s_mul_i32 s5, s5, 0x56000\n"
    "s_add_u32 s63, s5, s4\n"
    "buffer_load_dword v254, v160, s[24:27], 0 offen\n"
    "s_mul_i32 s81, s3, 0xc00\n"
    "v_lshrrev_b32 v2, 1, v251\n"
    "v_mul_u32_u24 v2, 0xc00, v2\n"
    "v_and_b32 v3, 1, v251\n"
    "v_lshl_add_u32 v3, v3, 2, v243\n"
    "v_lshl_add_u32 v2, v3, 7, v2\n"
    "global_load_ushort v38, v246, s[44:45]\n"
    "v_xor_b32 v3, s3, v234\n"
    "v_xor_b32 v4, s90, v243\n"
    "v_and_b32 v4, 1, v4\n"
    "v_lshlrev_b32 v5, 1, v4\n"
    "global_load_ushort v39, v246, s[46:47]\n"
    "v_xor_b32 v3, v3, v5\n"
    "v_lshl_add_u32 v2, v3, 5, v2\n"
    "v_add_u32 v221, v2, v242\n"
    "s_xor_b32 s4, s11, 1\n"
    "global_load_ushort v40, v246, s[48:49]\n"
    "v_and_b32 v4, s4, v4\n"
    "v_lshlrev_b32 v5, 10, v4\n"
    "v_sub_u32 v220, v221, v5\n"
    "v_add_u32 v220, 0x400, v220\n"
    "global_load_ushort v41, v246, s[50:51]\n"
    "v_lshl_add_u32 v221, v4, 11, v221\n"
    "v_lshrrev_b32 v2, 1, v244\n"
    "v_xor_b32 v3, v2, v244\n"
    "v_lshrrev_b32 v3, 1, v3\n"
    "global_load_ushort v42, v246, s[52:53]\n"
    "v_and_b32 v3, 2, v3\n"
    "v_and_b32 v2, 1, v2\n"
    "v_or_b32 v2, v2, v3\n"
    "v_lshlrev_b32 v3, 3, v251\n"
    "global_load_ushort v43, v246, s[54:55]\n"
    "v_lshl_add_u32 v3, v244, 7, v3\n"
    "s_lshr_b32 s4, s80, 1\n"
    "s_add_u32 s4, s4, 0x26000\n"
    "v_lshl_add_u32 v218, v2, 5, v3\n"
    "global_load_ushort v44, v233, s[56:57]\n"
    "v_add_u32 v218, s4, v218\n"
    "v_xor_b32 v2, s3, v2\n"
    "v_lshl_add_u32 v217, v2, 5, v3\n"
    "v_add_u32 v217, 0x26000, v217\n"
    "s_lshl1_add_u32 s4, s10, s11\n"
    "s_xor_b32 s4, s4, s3\n"
    "global_load_ushort v45, v233, s[58:59]\n"
    "s_lshl_b32 s4, s4, 5\n"
    "v_xor_b32 v216, s4, v18\n"
    "v_add_u32 v219, s80, v216\n"
    "v_add_u32 v216, 0x26000, v216\n"
    "buffer_load_dwordx2 v[230:231], v245, s[36:39], 0 offen sc1\n"
    "s_lshl_b32 s64, s69, 8\n"
    "s_add_u32 m0, s15, 0x1000\n"
    "v_and_b32 v2, 2, v255\n"
    "v_mul_u32_u24 v2, 0x7f8, v2\n"
    "v_add_u32 v216, v2, v216\n"
    "v_xor_b32 v215, 64, v216\n"
    "v_lshlrev_b32 v214, 5, v251\n"
    "v_add_u32 v214, 0x20c10, v214\n"
    "v_and_b32 v2, 8, v255\n"
    "v_lshl_add_u32 v214, v2, 4, v214\n"
    "s_lshl_b32 s4, s3, 6\n"
    "s_add_u32 s4, s4, 0x20c10\n"
    "buffer_load_dwordx4 v248, s[16:19], s64 offen lds\n"
    "s_add_u32 m0, s15, 0x1400\n"
    "v_lshl_add_u32 v213, v244, 2, s4\n"
    "s_lshl2_add_u32 s4, s6, s3\n"
    "s_lshl_b32 s4, s4, 16\n"
    "s_lshl_b32 s5, s8, 7\n"
    "s_add_u32 s4, s4, s5\n"
    "s_add_u32 s91, s4, 0x440000\n"
    "v_xor_b32 v208, 32, v218\n"
    "v_xor_b32 v207, 64, v218\n"
    "v_xor_b32 v206, 0x60, v218\n"
    "v_lshlrev_b32 v212, 4, v251\n"
    "v_lshl_add_u32 v212, v2, 3, v212\n"
    "buffer_load_dwordx4 v249, s[16:19], s64 offen lds\n"
    "s_add_u32 m0, s15, 0x1780\n"
    "v_and_b32 v2, 7, v255\n"
    "v_lshl_add_u32 v212, v2, 12, v212\n"
    "s_mul_i32 s4, s3, 0x1400\n"
    "s_add_u32 s4, s4, 0x21000\n"
    "v_mul_u32_u24 v211, 0x50, v255\n"
    "v_add_u32 v211, s4, v211\n"
    "v_and_b32 v2, 2, v251\n"
    "v_and_b32 v3, 1, v251\n"
    "v_lshl_add_u32 v2, v3, 3, v2\n"
    "v_and_b32 v3, 2, v243\n"
    "v_lshl_add_u32 v2, v3, 1, v2\n"
    "v_mul_u32_u24 v2, 0x50, v2\n"
    "v_and_b32 v3, 1, v243\n"
    "v_lshl_add_u32 v3, v3, 5, v242\n"
    "v_add3_u32 v210, v2, v3, s4\n"
    "v_and_b32 v3, 1, v251\n"
    "v_lshrrev_b32 v4, 1, v251\n"
    "v_lshlrev_b32 v2, 5, v244\n"
    "v_lshl_add_u32 v2, v3, 4, v2\n"
    "v_lshl_add_u32 v2, v4, 9, v2\n"
    "v_add_u32 v205, s4, v2\n"
    "v_lshlrev_b32 v2, 4, v244\n"
    "v_lshlrev_b32 v5, 3, v244\n"
    "v_add_u32 v5, 0x200, v5\n"
    "v_cmp_le_u32 vcc, 2, v251\n"
    "s_nop 1\n"
    "v_cndmask_b32 v2, v2, v5, vcc\n"
    "v_lshl_add_u32 v2, v3, 8, v2\n"
    "v_add_u32 v204, s4, v2\n"
    "v_mov_b32 v5, 8\n"
    "v_mov_b32 v3, 0x80\n"
    "v_cndmask_b32 v5, v5, v3, vcc\n"
    "v_add_u32 v203, v5, v204\n"
    "v_lshlrev_b32 v3, 2, v244\n"
    "v_lshl_add_u32 v3, v251, 8, v3\n"
    "v_add_u32 v202, s4, v3\n"
    "v_mov_b32 v54, 0\n"
    "v_mov_b32 v55, 0\n"
    "v_mov_b32 v56, 0\n"
    "v_mov_b32 v57, 0\n"
    "v_mov_b32 v201, s4\n"
    "s_lshl_b32 s4, s10, 6\n"
    "v_add_u32 v6, s4, v255\n"
    "s_mul_i32 s4, s11, 0x2800\n"
    "s_add_u32 s4, s4, 0x21010\n"
    "v_lshl_add_u32 v200, v6, 3, s4\n"
    "v_subrev_u32 v199, 4, v200\n"
    "v_lshl_add_u32 v198, v6, 2, s4\n"
    "v_and_b32 v2, 1, v6\n"
    "v_cmp_eq_u32 s[94:95], 1, v2\n"
    "v_lshlrev_b32 v2, 2, v2\n"
    "v_add_u32 v197, v198, v2\n"
    "v_sub_u32 v196, v198, v2\n"
    "v_subrev_u32 v3, 4, v196\n"
    "v_cmp_eq_u32 vcc, 0, v6\n"
    "s_nop 1\n"
    "v_cndmask_b32 v196, v196, v3, vcc\n"
    "s_mul_i32 s92, s11, 0x2080\n"
    "s_add_u32 s92, s92, 0x21000\n"
    "s_mul_i32 s93, s11, 0x11e0\n"
    "s_add_u32 s93, s93, 0x25100\n"
    "v_lshlrev_b32 v195, 2, v6\n"
    "v_sub_u32 v195, s92, v195\n"
    "v_lshlrev_b32 v194, 3, v6\n"
    "v_sub_u32 v194, s92, v194\n"
    "v_lshlrev_b32 v193, 4, v6\n"
    "v_sub_u32 v193, s92, v193\n"
    "s_mul_i32 s4, s10, 0x1040\n"
    "s_add_u32 s4, s4, s92\n"
    "v_and_b32 v192, 1, v255\n"
    "v_lshl_add_u32 v192, v192, 4, s4\n"
    "v_min_u32 v191, 29, v255\n"
    "v_lshl_add_u32 v191, v191, 4, s93\n"
    "v_lshl_add_u32 v2, v251, 3, v244\n"
    "v_sub_u32 v2, 0x807, v2\n"
    "v_lshrrev_b32 v2, 1, v2\n"
    "v_lshlrev_b32 v2, 2, v2\n"
    "v_and_b32 v3, 1, v244\n"
    "v_xor_b32 v3, 1, v3\n"
    "v_mul_u32_u24 v3, 0x1040, v3\n"
    "s_lshl_b32 s4, s10, 8\n"
    "s_sub_u32 s4, s92, s4\n"
    "v_add3_u32 v2, v2, v3, s4\n"
    "v_subrev_u32 v58, 0x2c0, v2\n"
    "v_subrev_u32 v59, 0x6c0, v2\n"
    "v_subrev_u32 v60, 0xac0, v2\n"
    "v_subrev_u32 v61, 0xec0, v2\n"
    "v_lshlrev_b32 v2, 5, v244\n"
    "v_lshlrev_b32 v3, 4, v251\n"
    "v_sub_u32 v2, v2, v3\n"
    "s_lshl_b32 s4, s10, 8\n"
    "s_sub_u32 s4, s93, s4\n"
    "s_add_u32 s4, s4, 0x130\n"
    "v_add_u32 v209, s4, v2\n"
    "s_lshl_b32 s4, s3, 5\n"
    "v_xor_b32 v2, s4, v235\n"
    "s_add_u32 s4, s80, 0x10800\n"
    "v_add_u32 v62, s4, v2\n"
    "v_xor_b32 v63, 32, v62\n"
    "v_xor_b32 v64, 64, v62\n"
    "v_xor_b32 v65, 0x60, v62\n"
    "buffer_load_dwordx4 v248, s[16:19], s64 offen offset:128 lds\n"
    "s_add_u32 m0, s15, 0x1b80\n"
    "s_nop 0\n"
    "buffer_load_dwordx4 v249, s[16:19], s64 offen offset:128 lds\n"
    "s_lshl2_add_u32 s64, s69, s3\n"
    "s_lshl_b32 s64, s64, 15\n"
    "s_add_u32 m0, s80, 0x4000\n"
    "s_nop 0\n"
    "buffer_load_dwordx4 v10, s[20:23], s64 offen lds\n"
    "s_waitcnt vmcnt(19)\n"
    "buffer_load_dwordx4 v11, s[20:23], s64 offen offset:1024 lds\n"
    "ds_read_b128 a[192:195], v14\n"
    "ds_read_b128 a[196:199], v15\n"
    "ds_read_b128 a[200:203], v14 offset:2048\n"
    "ds_read_b128 a[204:207], v15 offset:2048\n"
    "buffer_load_dwordx4 v12, s[20:23], s64 offen offset:2048 lds\n"
    "s_waitcnt lgkmcnt(3)\n"
    "v_mfma_f32_16x16x32_bf16 v[134:137], v[22:25], a[192:195], 0\n"
    "v_mfma_f32_16x16x32_bf16 v[138:141], a[192:195], a[192:195], 0\n"
    "s_waitcnt lgkmcnt(1)\n"
    "v_mfma_f32_16x16x32_bf16 v[134:137], v[22:25], a[196:199], v[134:137]\n"
    "buffer_load_dwordx4 v13, s[20:23], s64 offen offset:3072 lds\n"
    "v_mfma_f32_16x16x32_bf16 v[138:141], a[196:199], a[196:199], v[138:141]\n"
    "v_mfma_f32_16x16x32_bf16 v[134:137], v[22:25], a[200:203], v[134:137]\n"
    "v_mfma_f32_16x16x32_bf16 v[138:141], a[200:203], a[200:203], v[138:141]\n"
    "s_waitcnt lgkmcnt(0)\n"
    "v_mfma_f32_16x16x32_bf16 v[134:137], v[22:25], a[204:207], v[134:137]\n"
    "v_mfma_f32_16x16x32_bf16 v[138:141], a[204:207], a[204:207], v[138:141]\n"
    "buffer_load_dwordx4 v[34:37], v252, s[16:19], s66 offen\n"
    "s_lshl2_add_u32 s64, s8, s3\n"
    "s_lshl_b32 s64, s64, 14\n"
    "s_lshr_b32 s65, s80, 1\n"
    "s_add_u32 m0, s65, 0x26000\n"
    "v_mov_b32 v2, 0\n"
    "v_mov_b32 v3, 0\n"
    "buffer_load_dwordx4 v10, s[16:19], s64 offen lds\n"
    "s_waitcnt vmcnt(20)\n"
    "s_barrier\n"
    "ds_read_b64_tr_b16 a[224:225], v18\n"
    "ds_read_b64_tr_b16 a[226:227], v20 offset:512\n"
    "ds_read_b64_tr_b16 a[228:229], v19\n"
    "ds_read_b64_tr_b16 a[230:231], v21 offset:512\n"
    "ds_read_b128 a[176:179], v16\n"
    "buffer_load_dwordx4 v11, s[16:19], s64 offen offset:1024 lds\n"
    "s_lshl_b32 s64, s70, 8\n"
    "s_add_u32 m0, s15, 0x2000\n"
    "ds_read_b64_tr_b16 a[232:233], v18 offset:4096\n"
    "ds_read_b64_tr_b16 a[234:235], v20 offset:4608\n"
    "ds_read_b64_tr_b16 a[236:237], v19 offset:4096\n"
    "ds_read_b64_tr_b16 a[238:239], v21 offset:4608\n"
    "ds_read_b128 a[180:183], v17\n"
    "ds_read_b64_tr_b16 a[240:241], v18 offset:8192\n"
    "ds_read_b64_tr_b16 a[242:243], v20 offset:8704\n"
    "s_waitcnt lgkmcnt(10)\n"
    "v_mfma_f32_16x16x32_bf16 v[114:117], a[224:227], a[192:195], 0\n"
    "ds_read_b64_tr_b16 a[244:245], v19 offset:8192\n"
    "ds_read_b64_tr_b16 a[246:247], v21 offset:8704\n"
    "ds_read_b128 a[184:187], v16 offset:2048\n"
    "s_waitcnt lgkmcnt(10)\n"
    "v_mfma_f32_16x16x32_bf16 v[122:125], a[224:227], a[176:179], 0\n"
    "ds_read_b64_tr_b16 a[248:249], v18 offset:12288\n"
    "v_mfma_f32_16x16x32_bf16 v[118:121], a[228:231], a[192:195], 0\n"
    "ds_read_b64_tr_b16 a[250:251], v20 offset:12800\n"
    "v_mfma_f32_16x16x32_bf16 v[126:129], a[228:231], a[176:179], 0\n"
    "ds_read_b64_tr_b16 a[252:253], v19 offset:12288\n"
    "v_mfma_f32_16x16x32_bf16 v[130:133], a[224:227], v[22:25], 0\n"
    "ds_read_b64_tr_b16 a[254:255], v21 offset:12800\n"
    "s_waitcnt lgkmcnt(9)\n"
    "v_mfma_f32_16x16x32_bf16 v[114:117], a[232:235], a[196:199], v[114:117]\n"
    "ds_read_b128 a[188:191], v17 offset:2048\n"
    "v_mfma_f32_16x16x32_bf16 v[122:125], a[232:235], a[180:183], v[122:125]\n"
    "v_mfma_f32_16x16x32_bf16 v[118:121], a[236:239], a[196:199], v[118:121]\n"
    "v_mfma_f32_16x16x32_bf16 v[126:129], a[236:239], a[180:183], v[126:129]\n"
    "v_mfma_f32_16x16x32_bf16 v[130:133], a[232:235], v[22:25], v[130:133]\n"
    "s_waitcnt lgkmcnt(3)\n"
    "v_mfma_f32_16x16x32_bf16 v[114:117], a[240:243], a[200:203], v[114:117]\n"
    "v_mfma_f32_16x16x32_bf16 v[122:125], a[240:243], a[184:187], v[122:125]\n"
    "v_mfma_f32_16x16x32_bf16 v[118:121], a[244:247], a[200:203], v[118:121]\n"
    "buffer_load_dwordx4 v248, s[16:19], s64 offen lds\n"
    "s_add_u32 m0, s15, 0x2400\n"
    "v_mfma_f32_16x16x32_bf16 v[126:129], a[244:247], a[184:187], v[126:129]\n"
    "v_mfma_f32_16x16x32_bf16 v[130:133], a[240:243], v[22:25], v[130:133]\n"
    "v_mfma_f32_16x16x32_bf16 v[114:117], a[248:251], a[204:207], v[114:117]\n"
    "s_waitcnt lgkmcnt(0)\n"
    "v_mfma_f32_16x16x32_bf16 v[122:125], a[248:251], a[188:191], v[122:125]\n"
    "buffer_load_dwordx4 v249, s[16:19], s64 offen lds\n"
    "s_add_u32 m0, s15, 0x2780\n"
    "v_mfma_f32_16x16x32_bf16 v[118:121], a[252:255], a[204:207], v[118:121]\n"
    "v_mfma_f32_16x16x32_bf16 v[126:129], a[252:255], a[188:191], v[126:129]\n"
    "v_mfma_f32_16x16x32_bf16 v[130:133], a[248:251], v[22:25], v[130:133]\n"
    "buffer_load_dwordx4 v248, s[16:19], s64 offen offset:128 lds\n"
    "s_add_u32 m0, s15, 0x2b80\n"
    "s_nop 0\n"
    "buffer_load_dwordx4 v249, s[16:19], s64 offen offset:128 lds\n"
    "s_lshl2_add_u32 s64, s70, s3\n"
    "s_lshl_b32 s64, s64, 15\n"
    "s_add_u32 m0, s80, 0x8000\n"
    "s_waitcnt vmcnt(11)\n"
    "buffer_load_dwordx4 v10, s[20:23], s64 offen lds\n"
    "ds_read_b128 a[208:211], v14 offset:4096\n"
    "ds_read_b128 a[212:215], v15 offset:4096\n"
    "ds_read_b128 a[216:219], v14 offset:6144\n"
    "ds_read_b128 a[220:223], v15 offset:6144\n"
    "buffer_load_dwordx4 v11, s[20:23], s64 offen offset:1024 lds\n"
    "s_waitcnt lgkmcnt(3)\n"
    "v_mfma_f32_16x16x32_bf16 v[134:137], v[22:25], a[208:211], v[134:137]\n"
    "v_mfma_f32_16x16x32_bf16 v[138:141], a[208:211], a[208:211], v[138:141]\n"
    "s_waitcnt lgkmcnt(1)\n"
    "v_mfma_f32_16x16x32_bf16 v[134:137], v[22:25], a[212:215], v[134:137]\n"
    "buffer_load_dwordx4 v12, s[20:23], s64 offen offset:2048 lds\n"
    "v_mfma_f32_16x16x32_bf16 v[138:141], a[212:215], a[212:215], v[138:141]\n"
    "v_mfma_f32_16x16x32_bf16 v[134:137], v[22:25], a[216:219], v[134:137]\n"
    "v_mfma_f32_16x16x32_bf16 v[138:141], a[216:219], a[216:219], v[138:141]\n"
    "s_waitcnt lgkmcnt(0)\n"
    "v_mfma_f32_16x16x32_bf16 v[134:137], v[22:25], a[220:223], v[134:137]\n"
    "v_mfma_f32_16x16x32_bf16 v[138:141], a[220:223], a[220:223], v[138:141]\n"
    "buffer_load_dwordx4 v13, s[20:23], s64 offen offset:3072 lds\n"
    "s_lshl_b32 s64, s71, 8\n"
    "s_add_u32 m0, s15, 0x3000\n"
    "s_waitcnt vmcnt(11)\n"
    "buffer_load_dwordx4 v248, s[16:19], s64 offen lds\n"
    "s_add_u32 m0, s15, 0x3400\n"
    "s_barrier\n"
    "ds_read_b64_tr_b16 a[224:225], v18 offset:16384\n"
    "ds_read_b64_tr_b16 a[226:227], v20 offset:16896\n"
    "ds_read_b64_tr_b16 a[228:229], v19 offset:16384\n"
    "ds_read_b64_tr_b16 a[230:231], v21 offset:16896\n"
    "ds_read_b128 a[176:179], v16 offset:4096\n"
    "buffer_load_dwordx4 v249, s[16:19], s64 offen lds\n"
    "s_add_u32 m0, s15, 0x3780\n"
    "ds_read_b64_tr_b16 a[232:233], v18 offset:20480\n"
    "ds_read_b64_tr_b16 a[234:235], v20 offset:20992\n"
    "ds_read_b64_tr_b16 a[236:237], v19 offset:20480\n"
    "ds_read_b64_tr_b16 a[238:239], v21 offset:20992\n"
    "ds_read_b128 a[180:183], v17 offset:4096\n"
    "s_waitcnt vmcnt(12)\n"
    "v_dot2c_f32_bf16 v2, v34, v22\n"
    "ds_read_b64_tr_b16 a[240:241], v18 offset:24576\n"
    "buffer_load_dwordx4 v248, s[16:19], s64 offen offset:128 lds\n"
    "s_add_u32 m0, s15, 0x3b80\n"
    "s_waitcnt lgkmcnt(9)\n"
    "v_mfma_f32_16x16x32_bf16 v[114:117], a[224:227], a[208:211], v[114:117]\n"
    "ds_read_b64_tr_b16 a[242:243], v20 offset:25088\n"
    "ds_read_b64_tr_b16 a[244:245], v19 offset:24576\n"
    "ds_read_b64_tr_b16 a[246:247], v21 offset:25088\n"
    "s_waitcnt lgkmcnt(9)\n"
    "v_mfma_f32_16x16x32_bf16 v[122:125], a[224:227], a[176:179], v[122:125]\n"
    "ds_read_b128 a[184:187], v16 offset:6144\n"
    "buffer_load_dwordx4 v249, s[16:19], s64 offen offset:128 lds\n"
    "s_lshl2_add_u32 s64, s71, s3\n"
    "s_lshl_b32 s64, s64, 15\n"
    "s_add_u32 m0, s80, 0xc000\n"
    "v_mfma_f32_16x16x32_bf16 v[118:121], a[228:231], a[208:211], v[118:121]\n"
    "ds_read_b64_tr_b16 a[248:249], v18 offset:28672\n"
    "v_mfma_f32_16x16x32_bf16 v[126:129], a[228:231], a[176:179], v[126:129]\n"
    "ds_read_b64_tr_b16 a[250:251], v20 offset:29184\n"
    "v_mfma_f32_16x16x32_bf16 v[130:133], a[224:227], v[22:25], v[130:133]\n"
    "ds_read_b64_tr_b16 a[252:253], v19 offset:28672\n"
    "s_waitcnt lgkmcnt(7)\n"
    "v_mfma_f32_16x16x32_bf16 v[114:117], a[232:235], a[212:215], v[114:117]\n"
    "ds_read_b64_tr_b16 a[254:255], v21 offset:29184\n"
    "v_mfma_f32_16x16x32_bf16 v[122:125], a[232:235], a[180:183], v[122:125]\n"
    "ds_read_b128 a[188:191], v17 offset:6144\n"
    "v_mfma_f32_16x16x32_bf16 v[118:121], a[236:239], a[212:215], v[118:121]\n"
    "buffer_load_dwordx4 v10, s[20:23], s64 offen lds\n"
    "v_mfma_f32_16x16x32_bf16 v[126:129], a[236:239], a[180:183], v[126:129]\n"
    "v_mfma_f32_16x16x32_bf16 v[130:133], a[232:235], v[22:25], v[130:133]\n"
    "s_waitcnt lgkmcnt(4)\n"
    "v_mfma_f32_16x16x32_bf16 v[114:117], a[240:243], a[216:219], v[114:117]\n"
    "v_mfma_f32_16x16x32_bf16 v[122:125], a[240:243], a[184:187], v[122:125]\n"
    "buffer_load_dwordx4 v11, s[20:23], s64 offen offset:1024 lds\n"
    "v_mfma_f32_16x16x32_bf16 v[118:121], a[244:247], a[216:219], v[118:121]\n"
    "v_mfma_f32_16x16x32_bf16 v[126:129], a[244:247], a[184:187], v[126:129]\n"
    "v_mfma_f32_16x16x32_bf16 v[130:133], a[240:243], v[22:25], v[130:133]\n"
    "s_waitcnt lgkmcnt(0)\n"
    "v_mfma_f32_16x16x32_bf16 v[114:117], a[248:251], a[220:223], v[114:117]\n"
    "buffer_load_dwordx4 v12, s[20:23], s64 offen offset:2048 lds\n"
    "v_mfma_f32_16x16x32_bf16 v[122:125], a[248:251], a[188:191], v[122:125]\n"
    "v_mfma_f32_16x16x32_bf16 v[118:121], a[252:255], a[220:223], v[118:121]\n"
    "v_mfma_f32_16x16x32_bf16 v[126:129], a[252:255], a[188:191], v[126:129]\n"
    "v_mfma_f32_16x16x32_bf16 v[130:133], a[248:251], v[22:25], v[130:133]\n"
    "buffer_load_dwordx4 v13, s[20:23], s64 offen offset:3072 lds\n"
    "s_sub_u32 s64, 0x180, s24\n"
    "s_and_b32 s64, s64, 0x380\n"
    "s_waitcnt vmcnt(12)\n"
    "ds_read_b128 a[192:195], v14 offset:8192\n"
    "v_dot2c_f32_bf16 v3, v34, v34\n"
    "ds_read_b128 a[196:199], v15 offset:8192\n"
    "v_dot2c_f32_bf16 v2, v35, v22\n"
    "ds_read_b128 a[200:203], v14 offset:10240\n"
    "v_dot2c_f32_bf16 v3, v35, v35\n"
    "ds_read_b128 a[204:207], v15 offset:10240\n"
    "v_dot2c_f32_bf16 v2, v36, v22\n"
    "v_dot2c_f32_bf16 v3, v36, v36\n"
    "v_dot2c_f32_bf16 v2, v37, v22\n"
    "buffer_load_dword v254, v253, s[24:27], s64 offen\n"
    "v_dot2c_f32_bf16 v3, v37, v37\n"
    "s_add_u32 s4, s14, s9\n"
    "s_lshl_b32 s4, s4, 2\n"
    "s_add_u32 s4, s4, 0x20c10\n"
    "v_mov_b32 v6, s4\n"
    "v_permlane32_swap_b32 v2, v3\n"
    "v_add_f32 v2, v2, v3\n"
    "v_mov_b32 v3, v2\n"
    "s_waitcnt lgkmcnt(3)\n"
    "v_mfma_f32_16x16x32_bf16 v[134:137], v[22:25], a[192:195], v[134:137]\n"
    "v_permlane16_swap_b32 v2, v3\n"
    "v_mfma_f32_16x16x32_bf16 v[138:141], a[192:195], a[192:195], v[138:141]\n"
    "v_add_f32 v2, v2, v3\n"
    "s_waitcnt lgkmcnt(1)\n"
    "v_mfma_f32_16x16x32_bf16 v[134:137], v[22:25], a[196:199], v[134:137]\n"
    "buffer_load_dword v254, v161, s[28:31], 0 offen offset:2048\n"
    "v_mfma_f32_16x16x32_bf16 v[138:141], a[196:199], a[196:199], v[138:141]\n"
    "v_add_f32_dpp v2, v2, v2 row_ror:8 row_mask:0xf bank_mask:0xf\n"
    "v_mfma_f32_16x16x32_bf16 v[134:137], v[22:25], a[200:203], v[134:137]\n"
    "s_nop 0\n"
    "v_add_f32_dpp v2, v2, v2 row_ror:4 row_mask:0xf bank_mask:0xf\n"
    "v_mfma_f32_16x16x32_bf16 v[138:141], a[200:203], a[200:203], v[138:141]\n"
    "s_waitcnt lgkmcnt(0)\n"
    "v_mfma_f32_16x16x32_bf16 v[134:137], v[22:25], a[204:207], v[134:137]\n"
    "v_add_f32_dpp v2, v2, v2 row_ror:2 row_mask:0xf bank_mask:0xf\n"
    "v_mfma_f32_16x16x32_bf16 v[138:141], a[204:207], a[204:207], v[138:141]\n"
    "s_nop 0\n"
    "v_add_f32_dpp v2, v2, v2 row_ror:1 row_mask:0xf bank_mask:0xf\n"
    "v_mov_b32 v3, v2\n"
    "s_nop 1\n"
    "v_permlane32_swap_b32 v2, v3\n"
    "v_mul_f32 v4, 0x3b000000, v2\n"
    "v_mul_f32 v5, v4, v4\n"
    "v_fma_f32 v5, v3, s76, -v5\n"
    "v_max_f32 v5, 0, v5\n"
    "v_add_f32 v2, s77, v5\n"
    "v_rsq_f32 v2, v2\n"
    "s_nop 0\n"
    "v_mul_f32 v3, -v4, v2\n"
    "ds_write2_b32 v6, v2, v3 offset0:0 offset1:72\n"
    "buffer_load_dword v254, v161, s[32:35], 0 offen\n"
    "s_bfe_u32 s64, s24, 0x30007\n"
    "s_add_u32 s64, s64, s6\n"
    "s_and_b32 s64, s64, 7\n"
    "s_cmp_eq_u32 s64, 3\n"
    "s_cselect_b32 s64, s7, 0\n"
    "s_add_u32 s64, s64, s12\n"
    "s_lshr_b32 s71, s6, 1\n"
    "s_add_u32 s64, s64, s71\n"
    "s_and_b32 s68, s64, 3\n"
    "s_add_u32 s69, s64, 1\n"
    "s_and_b32 s69, s69, 3\n"
    "s_add_u32 s70, s64, 2\n"
    "s_and_b32 s70, s70, 3\n"
    "s_add_u32 s71, s64, 3\n"
    "s_and_b32 s71, s71, 3\n"
    "s_lshl2_add_u32 s64, s68, s3\n"
    "s_lshl_b32 s64, s64, 15\n"
    "s_add_u32 m0, s15, 0\n"
    "s_waitcnt vmcnt(11)\n"
    "s_barrier\n"
    "ds_read_b64_tr_b16 a[224:225], v18 offset:32768\n"
    "ds_read_b64_tr_b16 a[226:227], v20 offset:33280\n"
    "ds_read_b64_tr_b16 a[228:229], v19 offset:32768\n"
    "ds_read_b64_tr_b16 a[230:231], v21 offset:33280\n"
    "ds_read_b128 a[176:179], v16 offset:8192\n"
    "ds_read_b64_tr_b16 a[232:233], v18 offset:36864\n"
    "ds_read_b64_tr_b16 a[234:235], v20 offset:37376\n"
    "ds_read_b64_tr_b16 a[236:237], v19 offset:36864\n"
    "ds_read_b64_tr_b16 a[238:239], v21 offset:37376\n"
    "ds_read_b128 a[180:183], v17 offset:8192\n"
    "ds_read_b64_tr_b16 a[240:241], v18 offset:40960\n"
    "buffer_load_dwordx4 v10, s[24:27], s64 offen lds\n"
    "ds_read_b64_tr_b16 a[242:243], v20 offset:41472\n"
    "ds_read_b64_tr_b16 a[244:245], v19 offset:40960\n"
    "s_waitcnt lgkmcnt(11)\n"
    "v_mfma_f32_16x16x32_bf16 v[114:117], a[224:227], a[192:195], v[114:117]\n"
    "ds_read_b64_tr_b16 a[246:247], v21 offset:41472\n"
    "ds_read_b128 a[184:187], v16 offset:10240\n"
    "s_waitcnt lgkmcnt(10)\n"
    "v_mfma_f32_16x16x32_bf16 v[122:125], a[224:227], a[176:179], v[122:125]\n"
    "buffer_load_dwordx4 v11, s[24:27], s64 offen offset:1024 lds\n"
    "v_mfma_f32_16x16x32_bf16 v[118:121], a[228:231], a[192:195], v[118:121]\n"
    "ds_read_b64_tr_b16 a[248:249], v18 offset:45056\n"
    "s_waitcnt vmcnt(9)\n"
    "v_mfma_f32_16x16x32_bf16 v[126:129], a[228:231], a[176:179], v[126:129]\n"
    "ds_read_b64_tr_b16 a[250:251], v20 offset:45568\n"
    "v_mfma_f32_16x16x32_bf16 v[130:133], a[224:227], v[22:25], v[130:133]\n"
    "ds_read_b64_tr_b16 a[252:253], v19 offset:45056\n"
    "s_waitcnt lgkmcnt(7)\n"
    "v_mfma_f32_16x16x32_bf16 v[114:117], a[232:235], a[196:199], v[114:117]\n"
    "ds_read_b64_tr_b16 a[254:255], v21 offset:45568\n"
    "v_mfma_f32_16x16x32_bf16 v[122:125], a[232:235], a[180:183], v[122:125]\n"
    "ds_read_b128 a[188:191], v17 offset:10240\n"
    "v_mfma_f32_16x16x32_bf16 v[118:121], a[236:239], a[196:199], v[118:121]\n"
    "ds_read_b128 a[208:211], v14 offset:12288\n"
    "v_mfma_f32_16x16x32_bf16 v[126:129], a[236:239], a[180:183], v[126:129]\n"
    "ds_read_b128 a[212:215], v15 offset:12288\n"
    "buffer_load_dwordx4 v12, s[24:27], s64 offen offset:2048 lds\n"
    "v_mfma_f32_16x16x32_bf16 v[130:133], a[232:235], v[22:25], v[130:133]\n"
    "ds_read_b128 a[216:219], v14 offset:14336\n"
    "s_waitcnt lgkmcnt(7)\n"
    "v_mfma_f32_16x16x32_bf16 v[114:117], a[240:243], a[200:203], v[114:117]\n"
    "ds_read_b128 a[220:223], v15 offset:14336\n"
    "v_mfma_f32_16x16x32_bf16 v[122:125], a[240:243], a[184:187], v[122:125]\n"
    "ds_read_b64_tr_b16 v[66:67], v216\n"
    "v_mfma_f32_16x16x32_bf16 v[118:121], a[244:247], a[200:203], v[118:121]\n"
    "ds_read_b64_tr_b16 v[68:69], v215 offset:512\n"
    "v_mfma_f32_16x16x32_bf16 v[126:129], a[244:247], a[184:187], v[126:129]\n"
    "ds_read_b64_tr_b16 v[70:71], v216 offset:16\n"
    "v_mfma_f32_16x16x32_bf16 v[130:133], a[240:243], v[22:25], v[130:133]\n"
    "buffer_load_dwordx4 v13, s[24:27], s64 offen offset:3072 lds\n"
    "s_add_u32 m0, s81, 0\n"
    "s_waitcnt lgkmcnt(6)\n"
    "v_mfma_f32_16x16x32_bf16 v[114:117], a[248:251], a[204:207], v[114:117]\n"
    "ds_read_b64_tr_b16 v[72:73], v215 offset:528\n"
    "v_mfma_f32_16x16x32_bf16 v[122:125], a[248:251], a[188:191], v[122:125]\n"
    "ds_read_b64 v[2:3], v218\n"
    "v_mfma_f32_16x16x32_bf16 v[118:121], a[252:255], a[204:207], v[118:121]\n"
    "ds_read_b64 v[4:5], v208\n"
    "v_mfma_f32_16x16x32_bf16 v[126:129], a[252:255], a[188:191], v[126:129]\n"
    "ds_read_b64 v[6:7], v207\n"
    "v_mfma_f32_16x16x32_bf16 v[130:133], a[248:251], v[22:25], v[130:133]\n"
    "ds_read_b64 v[8:9], v206\n"
    "buffer_load_dwordx4 v224, s[28:31], s63 offen lds\n"
    "v_mfma_f32_16x16x32_bf16 v[134:137], v[22:25], a[208:211], v[134:137]\n"
    "ds_read_b64 v[106:107], v217\n"
    "v_mfma_f32_16x16x32_bf16 v[138:141], a[208:211], a[208:211], v[138:141]\n"
    "ds_read_b64 v[108:109], v217 offset:2048\n"
    "s_waitcnt lgkmcnt(7)\n"
    "v_mfma_f32_16x16x32_bf16 v[134:137], v[22:25], a[212:215], v[134:137]\n"
    "ds_read_b64 v[110:111], v217 offset:4096\n"
    "v_mfma_f32_16x16x32_bf16 v[138:141], a[212:215], a[212:215], v[138:141]\n"
    "ds_read_b64 v[112:113], v217 offset:6144\n"
    "v_lshlrev_b32 v74, 16, v66\n"
    "v_mfma_f32_16x16x32_bf16 v[134:137], v[22:25], a[216:219], v[134:137]\n"
    "v_and_b32 v75, s78, v66\n"
    "v_mfma_f32_16x16x32_bf16 v[138:141], a[216:219], a[216:219], v[138:141]\n"
    "buffer_load_dwordx4 v225, s[28:31], s63 offen offset:1024 lds\n"
    "s_waitcnt vmcnt(9)\n"
    "s_barrier\n"
    "ds_read_b64_tr_b16 a[224:225], v18 offset:49152\n"
    "ds_read_b64_tr_b16 a[226:227], v20 offset:49664\n"
    "ds_read_b64_tr_b16 a[228:229], v19 offset:49152\n"
    "ds_read_b64_tr_b16 a[230:231], v21 offset:49664\n"
    "ds_read_b128 a[176:179], v16 offset:12288\n"
    "v_mfma_f32_16x16x32_bf16 v[134:137], v[22:25], a[220:223], v[134:137]\n"
    "ds_read_b64_tr_b16 a[232:233], v18 offset:53248\n"
    "ds_read_b64_tr_b16 a[234:235], v20 offset:53760\n"
    "ds_read_b64_tr_b16 a[236:237], v19 offset:53248\n"
    "ds_read_b64_tr_b16 a[238:239], v21 offset:53760\n"
    "ds_read_b128 a[180:183], v17 offset:12288\n"
    "v_mfma_f32_16x16x32_bf16 v[138:141], a[220:223], a[220:223], v[138:141]\n"
    "ds_read_b64_tr_b16 a[240:241], v18 offset:57344\n"
    "ds_read_b64_tr_b16 a[242:243], v20 offset:57856\n"
    "ds_read_b64_tr_b16 a[244:245], v19 offset:57344\n"
    "ds_read_b64_tr_b16 a[246:247], v21 offset:57856\n"
    "s_waitcnt lgkmcnt(12)\n"
    "v_mfma_f32_16x16x32_bf16 v[114:117], a[224:227], a[208:211], v[114:117]\n"
    "ds_read_b128 a[184:187], v16 offset:14336\n"
    "v_lshlrev_b32 v76, 16, v67\n"
    "ds_read_b64_tr_b16 a[248:249], v18 offset:61440\n"
    "s_waitcnt lgkmcnt(11)\n"
    "v_mfma_f32_16x16x32_bf16 v[122:125], a[224:227], a[176:179], v[122:125]\n"
    "ds_read_b64_tr_b16 a[250:251], v20 offset:61952\n"
    "v_mfma_f32_16x16x32_bf16 v[118:121], a[228:231], a[208:211], v[118:121]\n"
    "ds_read_b64_tr_b16 a[252:253], v19 offset:61440\n"
    "v_mfma_f32_16x16x32_bf16 v[126:129], a[228:231], a[176:179], v[126:129]\n"
    "ds_read_b64_tr_b16 a[254:255], v21 offset:61952\n"
    "v_mfma_f32_16x16x32_bf16 v[130:133], a[224:227], v[22:25], v[130:133]\n"
    "ds_read_b128 a[188:191], v17 offset:14336\n"
    "s_waitcnt lgkmcnt(8)\n"
    "v_mfma_f32_16x16x32_bf16 v[114:117], a[232:235], a[212:215], v[114:117]\n"
    "buffer_load_dwordx4 v226, s[28:31], s63 offen offset:2048 lds\n"
    "s_lshl2_add_u32 s64, s69, s3\n"
    "s_lshl_b32 s64, s64, 15\n"
    "s_add_u32 m0, s15, 0x1000\n"
    "v_mfma_f32_16x16x32_bf16 v[122:125], a[232:235], a[180:183], v[122:125]\n"
    "v_cndmask_b32 v154, v138, v139, s[72:73]\n"
    "v_cndmask_b32 v155, v140, v141, s[72:73]\n"
    "v_mfma_f32_16x16x32_bf16 v[118:121], a[236:239], a[212:215], v[118:121]\n"
    "v_cndmask_b32 v154, v154, v155, s[74:75]\n"
    "v_mul_f32 v156, 0x3b000000, v134\n"
    "v_mfma_f32_16x16x32_bf16 v[126:129], a[236:239], a[180:183], v[126:129]\n"
    "v_mul_f32 v157, v156, v156\n"
    "v_fma_f32 v157, v154, s76, -v157\n"
    "v_mfma_f32_16x16x32_bf16 v[130:133], a[232:235], v[22:25], v[130:133]\n"
    "v_max_f32 v157, 0, v157\n"
    "v_add_f32 v155, s77, v157\n"
    "v_mfma_f32_16x16x32_bf16 v[114:117], a[240:243], a[216:219], v[114:117]\n"
    "buffer_load_dwordx4 v10, s[24:27], s64 offen lds\n"
    "s_waitcnt lgkmcnt(0)\n"
    "v_mfma_f32_16x16x32_bf16 v[122:125], a[240:243], a[184:187], v[122:125]\n"
    "v_rsq_f32 v155, v155\n"
    "v_mfma_f32_16x16x32_bf16 v[118:121], a[244:247], a[216:219], v[118:121]\n"
    "v_mul_f32 v154, v155, v155\n"
    "v_fma_f32 v154, v154, v157, s77\n"
    "v_mfma_f32_16x16x32_bf16 v[126:129], a[244:247], a[184:187], v[126:129]\n"
    "v_rsq_f32 v154, v154\n"
    "v_mfma_f32_16x16x32_bf16 v[130:133], a[240:243], v[22:25], v[130:133]\n"
    "v_mul_f32 v157, -v156, v155\n"
    "v_mul_f32 v154, v154, v155\n"
    "v_mfma_f32_16x16x32_bf16 v[114:117], a[248:251], a[220:223], v[114:117]\n"
    "v_mul_f32 v158, 0xc0135761, v154\n"
    "v_mul_f32 v135, v154, v154\n"
    "v_mfma_f32_16x16x32_bf16 v[122:125], a[248:251], a[188:191], v[122:125]\n"
    "v_rcp_f32 v160, v154\n"
    "v_mfma_f32_16x16x32_bf16 v[118:121], a[252:255], a[220:223], v[118:121]\n"
    "v_mul_f32 v135, 0x3d372713, v135\n"
    "v_sub_f32 v161, 0, v156\n"
    "v_mfma_f32_16x16x32_bf16 v[126:129], a[252:255], a[188:191], v[126:129]\n"
    "v_mul_f32 v159, v135, v158\n"
    "v_and_b32 v77, s78, v67\n"
    "v_mfma_f32_16x16x32_bf16 v[130:133], a[248:251], v[22:25], v[130:133]\n"
    "ds_write2_b32 v240, v155, v157 offset0:0 offset1:72\n"
    "v_lshlrev_b32 v78, 16, v68\n"
    "ds_write_b128 v241, v[158:161]\n"
    "buffer_load_dwordx4 v11, s[24:27], s64 offen offset:1024 lds\n"
    "v_and_b32 v79, s78, v68\n"
    "v_lshlrev_b32 v80, 16, v69\n"
    "v_and_b32 v81, s78, v69\n"
    "v_lshlrev_b32 v82, 16, v70\n"
    "ds_read_b128 v[142:145], v238\n"
    "v_and_b32 v83, s78, v70\n"
    "v_lshlrev_b32 v84, 16, v71\n"
    "v_and_b32 v85, s78, v71\n"
    "v_lshlrev_b32 v86, 16, v72\n"
    "v_and_b32 v87, s78, v72\n"
    "buffer_load_dwordx4 v12, s[24:27], s64 offen offset:2048 lds\n"
    "v_lshlrev_b32 v88, 16, v73\n"
    "v_and_b32 v89, s78, v73\n"
    "v_lshlrev_b32 v90, 16, v2\n"
    "v_and_b32 v91, s78, v2\n"
    "v_lshlrev_b32 v92, 16, v3\n"
    "v_and_b32 v93, s78, v3\n"
    "v_lshlrev_b32 v94, 16, v4\n"
    "v_and_b32 v95, s78, v4\n"
    "v_lshlrev_b32 v96, 16, v5\n"
    "v_and_b32 v97, s78, v5\n"
    "v_lshlrev_b32 v98, 16, v6\n"
    "v_and_b32 v99, s78, v6\n"
    "buffer_load_dwordx4 v13, s[24:27], s64 offen offset:3072 lds\n"
    "s_add_u32 s64, s63, 0x15800\n"
    "s_add_u32 m0, s81, 0x3000\n"
    "v_lshlrev_b32 v100, 16, v7\n"
    "v_and_b32 v101, s78, v7\n"
    "v_lshlrev_b32 v102, 16, v8\n"
    "v_and_b32 v103, s78, v8\n"
    "v_lshlrev_b32 v104, 16, v9\n"
    "v_and_b32 v105, s78, v9\n"
    "v_readfirstlane_b32 s88, v230\n"
    "v_readfirstlane_b32 s89, v231\n"
    "s_nop 0\n"
    "s_add_u32 s4, s88, 1\n"
    "s_addc_u32 s5, s89, 0\n"
    "ds_write_b128 v236, v[130:133]\n"
    "s_add_u32 s88, s88, 0xd9b4e019\n"
    "s_addc_u32 s89, s89, 0x632be59b\n"
    "s_lshl_b32 s67, s10, 10\n"
    "s_add_u32 s67, s67, s60\n"
    "s_add_u32 s66, s67, 0x40000\n"
    "s_xor_b32 s67, s67, 0x4000\n"
    "s_add_u32 s67, s67, 0x40000\n"
    "buffer_load_dwordx4 v224, s[28:31], s64 offen lds\n"
    "buffer_load_dwordx4 v225, s[28:31], s64 offen offset:1024 lds\n"
    "s_waitcnt lgkmcnt(0)\n"
    "s_barrier\n"
    "ds_read_b128 v[150:153], v237\n"
    "ds_read_b128 v[146:149], v239\n"
    "v_pk_fma_f32 v[114:115], v[130:131], v[144:145], v[114:115] op_sel:[0,1,0] op_sel_hi:[1,1,1]\n"
    "v_pk_fma_f32 v[116:117], v[132:133], v[144:145], v[116:117] op_sel:[0,1,0] op_sel_hi:[1,1,1]\n"
    "v_pk_mul_f32 v[2:3], v[114:115], v[114:115]\n"
    "v_pk_mul_f32 v[4:5], v[116:117], v[116:117]\n"
    "v_pk_fma_f32 v[2:3], v[2:3], v[142:143], v[142:143] op_sel:[0,1,0] op_sel_hi:[1,1,0]\n"
    "v_pk_fma_f32 v[4:5], v[4:5], v[142:143], v[142:143] op_sel:[0,1,0] op_sel_hi:[1,1,0]\n"
    "v_pk_mul_f32 v[2:3], v[2:3], v[114:115]\n"
    "v_pk_mul_f32 v[4:5], v[4:5], v[116:117]\n"
    "v_exp_f32 v2, v2\n"
    "v_exp_f32 v3, v3\n"
    "v_exp_f32 v4, v4\n"
    "v_exp_f32 v5, v5\n"
    "v_pk_fma_f32 v[2:3], v[2:3], v[144:145], v[144:145] op_sel:[0,0,0] op_sel_hi:[1,0,0]\n"
    "v_pk_fma_f32 v[4:5], v[4:5], v[144:145], v[144:145] op_sel:[0,0,0] op_sel_hi:[1,0,0]\n"
    "v_rcp_f32 v2, v2\n"
    "v_rcp_f32 v3, v3\n"
    "v_rcp_f32 v4, v4\n"
    "v_rcp_f32 v5, v5\n"
    "v_pk_mul_f32 v[2:3], v[2:3], v[114:115]\n"
    "v_pk_mul_f32 v[4:5], v[4:5], v[116:117]\n"
    "v_cvt_pk_bf16_f32 v14, v2, v3\n"
    "v_cvt_pk_bf16_f32 v15, v4, v5\n"
    "s_waitcnt lgkmcnt(0)\n"
    "v_pk_fma_f32 v[118:119], v[150:151], v[144:145], v[118:119] op_sel:[0,1,0] op_sel_hi:[1,1,1]\n"
    "v_pk_fma_f32 v[120:121], v[152:153], v[144:145], v[120:121] op_sel:[0,1,0] op_sel_hi:[1,1,1]\n"
    "v_pk_fma_f32 v[122:123], v[130:131], v[148:149], v[122:123] op_sel:[0,1,0] op_sel_hi:[1,1,1]\n"
    "v_pk_fma_f32 v[124:125], v[132:133], v[148:149], v[124:125] op_sel:[0,1,0] op_sel_hi:[1,1,1]\n"
    "v_pk_mul_f32 v[2:3], v[118:119], v[118:119]\n"
    "v_pk_mul_f32 v[4:5], v[120:121], v[120:121]\n"
    "v_pk_mul_f32 v[6:7], v[122:123], v[122:123]\n"
    "v_pk_mul_f32 v[8:9], v[124:125], v[124:125]\n"
    "v_pk_fma_f32 v[2:3], v[2:3], v[142:143], v[142:143] op_sel:[0,1,0] op_sel_hi:[1,1,0]\n"
    "v_pk_fma_f32 v[4:5], v[4:5], v[142:143], v[142:143] op_sel:[0,1,0] op_sel_hi:[1,1,0]\n"
    "v_pk_fma_f32 v[6:7], v[6:7], v[146:147], v[146:147] op_sel:[0,1,0] op_sel_hi:[1,1,0]\n"
    "v_pk_fma_f32 v[8:9], v[8:9], v[146:147], v[146:147] op_sel:[0,1,0] op_sel_hi:[1,1,0]\n"
    "v_pk_mul_f32 v[2:3], v[2:3], v[118:119]\n"
    "v_pk_mul_f32 v[4:5], v[4:5], v[120:121]\n"
    "v_pk_mul_f32 v[6:7], v[6:7], v[122:123]\n"
    "v_pk_mul_f32 v[8:9], v[8:9], v[124:125]\n"
    "v_exp_f32 v2, v2\n"
    "v_exp_f32 v3, v3\n"
    "v_exp_f32 v4, v4\n"
    "v_exp_f32 v5, v5\n"
    "v_exp_f32 v6, v6\n"
    "v_exp_f32 v7, v7\n"
    "v_exp_f32 v8, v8\n"
    "v_exp_f32 v9, v9\n"
    "v_pk_fma_f32 v[2:3], v[2:3], v[144:145], v[144:145] op_sel:[0,0,0] op_sel_hi:[1,0,0]\n"
    "v_pk_fma_f32 v[4:5], v[4:5], v[144:145], v[144:145] op_sel:[0,0,0] op_sel_hi:[1,0,0]\n"
    "v_pk_fma_f32 v[6:7], v[6:7], v[148:149], v[148:149] op_sel:[0,0,0] op_sel_hi:[1,0,0]\n"
    "v_pk_fma_f32 v[8:9], v[8:9], v[148:149], v[148:149] op_sel:[0,0,0] op_sel_hi:[1,0,0]\n"
    "v_rcp_f32 v2, v2\n"
    "v_rcp_f32 v3, v3\n"
    "v_rcp_f32 v4, v4\n"
    "v_rcp_f32 v5, v5\n"
    "v_rcp_f32 v6, v6\n"
    "v_rcp_f32 v7, v7\n"
    "v_rcp_f32 v8, v8\n"
    "v_rcp_f32 v9, v9\n"
    "v_pk_mul_f32 v[2:3], v[2:3], v[118:119]\n"
    "v_pk_mul_f32 v[4:5], v[4:5], v[120:121]\n"
    "v_pk_mul_f32 v[6:7], v[6:7], v[122:123]\n"
    "v_pk_mul_f32 v[8:9], v[8:9], v[124:125]\n"
    "v_cvt_pk_bf16_f32 v16, v2, v3\n"
    "v_cvt_pk_bf16_f32 v17, v4, v5\n"
    "v_cvt_pk_bf16_f32 v18, v6, v7\n"
    "v_cvt_pk_bf16_f32 v19, v8, v9\n"
    "v_mov_b32 v222, s88\n"
    "v_mov_b32 v223, s89\n"
    "buffer_store_dwordx4 v[14:17], v252, s[36:39], s66 offen sc1\n"
    "v_pk_fma_f32 v[126:127], v[150:151], v[148:149], v[126:127] op_sel:[0,1,0] op_sel_hi:[1,1,1]\n"
    "v_pk_fma_f32 v[128:129], v[152:153], v[148:149], v[128:129] op_sel:[0,1,0] op_sel_hi:[1,1,1]\n"
    "v_pk_mul_f32 v[2:3], v[126:127], v[126:127]\n"
    "v_pk_mul_f32 v[4:5], v[128:129], v[128:129]\n"
    "v_pk_fma_f32 v[2:3], v[2:3], v[146:147], v[146:147] op_sel:[0,1,0] op_sel_hi:[1,1,0]\n"
    "v_pk_fma_f32 v[4:5], v[4:5], v[146:147], v[146:147] op_sel:[0,1,0] op_sel_hi:[1,1,0]\n"
    "v_pk_mul_f32 v[2:3], v[2:3], v[126:127]\n"
    "v_pk_mul_f32 v[4:5], v[4:5], v[128:129]\n"
    "v_exp_f32 v2, v2\n"
    "v_exp_f32 v3, v3\n"
    "v_exp_f32 v4, v4\n"
    "v_exp_f32 v5, v5\n"
    "v_pk_fma_f32 v[2:3], v[2:3], v[148:149], v[148:149] op_sel:[0,0,0] op_sel_hi:[1,0,0]\n"
    "v_pk_fma_f32 v[4:5], v[4:5], v[148:149], v[148:149] op_sel:[0,0,0] op_sel_hi:[1,0,0]\n"
    "v_rcp_f32 v2, v2\n"
    "v_rcp_f32 v3, v3\n"
    "v_rcp_f32 v4, v4\n"
    "v_rcp_f32 v5, v5\n"
    "v_pk_mul_f32 v[2:3], v[2:3], v[126:127]\n"
    "v_pk_mul_f32 v[4:5], v[4:5], v[128:129]\n"
    "v_cvt_pk_bf16_f32 v20, v2, v3\n"
    "v_cvt_pk_bf16_f32 v21, v4, v5\n"
    "buffer_store_dwordx4 v[18:21], v252, s[36:39], s67 offen sc1\n"
    "s_lshl1_add_u32 s20, s8, s11\n"
    "s_lshl_b32 s20, s20, 7\n"
    "s_add_u32 s20, s20, 0x1000\n"
    "s_lshl1_add_u32 s44, s6, s10\n"
    "s_lshl3_add_u32 s21, s44, s20\n"
    "ds_read_b128 v[122:125], v214\n"
    "v_lshlrev_b32 v38, 16, v38\n"
    "ds_read_b128 v[130:133], v214 offset:288\n"
    "v_lshlrev_b32 v39, 16, v39\n"
    "ds_read_b128 v[126:129], v214 offset:16\n"
    "v_lshlrev_b32 v40, 16, v40\n"
    "ds_read_b128 v[134:137], v214 offset:304\n"
    "v_lshlrev_b32 v41, 16, v41\n"
    "buffer_load_dwordx4 v226, s[28:31], s64 offen offset:2048 lds\n"
    "s_lshl2_add_u32 s64, s70, s3\n"
    "s_lshl_b32 s64, s64, 15\n"
    "s_add_u32 m0, s15, 0x2000\n"
    "ds_read2_b32 v[242:243], v213 offset0:0 offset1:72\n"
    "v_lshlrev_b32 v42, 16, v42\n"
    "v_lshlrev_b32 v43, 16, v43\n"
    "v_lshlrev_b32 v44, 16, v44\n"
    "v_lshlrev_b32 v45, 16, v45\n"
    "v_mul_f32 v44, 0x3fb8aa3b, v44\n"
    "v_exp_f32 v44, v44\n"
    "s_nop 0\n"
    "v_cvt_pk_bf16_f32 v44, v44, v44\n"
    "v_and_b32 v44, s78, v44\n"
    "v_pk_mul_f32 v[38:39], v[38:39], v[44:45] op_sel_hi:[1,0]\n"
    "v_pk_mul_f32 v[40:41], v[40:41], v[44:45] op_sel_hi:[1,0]\n"
    "v_mul_f32 v38, 0x3fb8aa3b, v38\n"
    "s_waitcnt lgkmcnt(3)\n"
    "v_pk_fma_f32 v[74:75], v[74:75], v[122:123], v[130:131]\n"
    "buffer_load_dwordx4 v10, s[24:27], s64 offen lds\n"
    "v_cvt_pk_bf16_f32 v66, v74, v75\n"
    "v_pk_fma_f32 v[76:77], v[76:77], v[124:125], v[132:133]\n"
    "v_cvt_pk_bf16_f32 v67, v76, v77\n"
    "s_waitcnt lgkmcnt(1)\n"
    "v_pk_fma_f32 v[78:79], v[78:79], v[126:127], v[134:135]\n"
    "v_cvt_pk_bf16_f32 v68, v78, v79\n"
    "v_pk_fma_f32 v[80:81], v[80:81], v[128:129], v[136:137]\n"
    "v_cvt_pk_bf16_f32 v69, v80, v81\n"
    "v_pk_fma_f32 v[82:83], v[82:83], v[122:123], v[130:131]\n"
    "v_cvt_pk_bf16_f32 v70, v82, v83\n"
    "v_pk_fma_f32 v[84:85], v[84:85], v[124:125], v[132:133]\n"
    "v_cvt_pk_bf16_f32 v71, v84, v85\n"
    "v_pk_fma_f32 v[86:87], v[86:87], v[126:127], v[134:135]\n"
    "v_cvt_pk_bf16_f32 v72, v86, v87\n"
    "v_pk_fma_f32 v[88:89], v[88:89], v[128:129], v[136:137]\n"
    "v_cvt_pk_bf16_f32 v73, v88, v89\n"
    "s_waitcnt lgkmcnt(0)\n"
    "v_pk_fma_f32 v[90:91], v[90:91], v[242:243], v[242:243] op_sel:[0,0,1] op_sel_hi:[1,0,1]\n"
    "v_pk_fma_f32 v[92:93], v[92:93], v[242:243], v[242:243] op_sel:[0,0,1] op_sel_hi:[1,0,1]\n"
    "v_pk_fma_f32 v[94:95], v[94:95], v[242:243], v[242:243] op_sel:[0,0,1] op_sel_hi:[1,0,1]\n"
    "buffer_load_dwordx4 v11, s[24:27], s64 offen offset:1024 lds\n"
    "v_pk_fma_f32 v[96:97], v[96:97], v[242:243], v[242:243] op_sel:[0,0,1] op_sel_hi:[1,0,1]\n"
    "v_pk_fma_f32 v[98:99], v[98:99], v[242:243], v[242:243] op_sel:[0,0,1] op_sel_hi:[1,0,1]\n"
    "v_pk_fma_f32 v[100:101], v[100:101], v[242:243], v[242:243] op_sel:[0,0,1] op_sel_hi:[1,0,1]\n"
    "v_pk_fma_f32 v[102:103], v[102:103], v[242:243], v[242:243] op_sel:[0,0,1] op_sel_hi:[1,0,1]\n"
    "v_pk_fma_f32 v[104:105], v[104:105], v[242:243], v[242:243] op_sel:[0,0,1] op_sel_hi:[1,0,1]\n"
    "v_mul_f32 v39, 0x3e22f983, v39\n"
    "v_mul_f32 v4, 0x41000000, v38\n"
    "v_mul_f32 v5, 0x41000000, v39\n"
    "v_mul_f32 v6, 0x43000000, v38\n"
    "v_mul_f32 v7, 0x43000000, v39\n"
    "v_exp_f32 v2, v38\n"
    "v_fract_f32 v3, v39\n"
    "v_exp_f32 v4, v4\n"
    "v_fract_f32 v5, v5\n"
    "v_exp_f32 v6, v6\n"
    "v_fract_f32 v7, v7\n"
    "buffer_load_dwordx4 v12, s[24:27], s64 offen offset:2048 lds\n"
    "v_cos_f32 v240, v3\n"
    "v_sin_f32 v241, v3\n"
    "v_cos_f32 v238, v5\n"
    "v_sin_f32 v239, v5\n"
    "v_cos_f32 v236, v7\n"
    "v_sin_f32 v237, v7\n"
    "v_pk_mul_f32 v[240:241], v[240:241], v[2:3] op_sel_hi:[1,0]\n"
    "v_pk_mul_f32 v[238:239], v[238:239], v[4:5] op_sel_hi:[1,0]\n"
    "v_pk_mul_f32 v[236:237], v[236:237], v[6:7] op_sel_hi:[1,0]\n"
    "v_pk_mul_f32 v[230:231], v[42:43], v[40:41] op_sel:[0,0] op_sel_hi:[0,1]\n"
    "v_pk_fma_f32 v[216:217], v[42:43], v[40:41], v[230:231] op_sel:[1,1,0] op_sel_hi:[1,0,1] neg_lo:[1,0,0]\n"
    "v_pk_mul_f32 v[230:231], v[216:217], v[238:239] op_sel:[0,0] op_sel_hi:[0,1]\n"
    "v_pk_fma_f32 v[206:207], v[216:217], v[238:239], v[230:231] op_sel:[1,1,0] op_sel_hi:[1,0,1] neg_lo:[1,0,0]\n"
    "v_cvt_pk_bf16_f32 v14, v216, v206\n"
    "buffer_load_dwordx4 v13, s[24:27], s64 offen offset:3072 lds\n"
    "s_lshl2_add_u32 s64, s71, s3\n"
    "s_lshl_b32 s64, s64, 15\n"
    "s_add_u32 m0, s15, 0x3000\n"
    "v_cvt_pk_bf16_f32 v138, -v217, -v207\n"
    "v_pk_mul_f32 v[230:231], v[206:207], v[238:239] op_sel:[0,0] op_sel_hi:[0,1]\n"
    "v_pk_fma_f32 v[216:217], v[206:207], v[238:239], v[230:231] op_sel:[1,1,0] op_sel_hi:[1,0,1] neg_lo:[1,0,0]\n"
    "v_pk_mul_f32 v[230:231], v[216:217], v[238:239] op_sel:[0,0] op_sel_hi:[0,1]\n"
    "v_pk_fma_f32 v[206:207], v[216:217], v[238:239], v[230:231] op_sel:[1,1,0] op_sel_hi:[1,0,1] neg_lo:[1,0,0]\n"
    "v_cvt_pk_bf16_f32 v15, v216, v206\n"
    "v_cvt_pk_bf16_f32 v139, -v217, -v207\n"
    "v_pk_mul_f32 v[230:231], v[206:207], v[238:239] op_sel:[0,0] op_sel_hi:[0,1]\n"
    "v_pk_fma_f32 v[216:217], v[206:207], v[238:239], v[230:231] op_sel:[1,1,0] op_sel_hi:[1,0,1] neg_lo:[1,0,0]\n"
    "v_pk_mul_f32 v[230:231], v[216:217], v[238:239] op_sel:[0,0] op_sel_hi:[0,1]\n"
    "v_pk_fma_f32 v[206:207], v[216:217], v[238:239], v[230:231] op_sel:[1,1,0] op_sel_hi:[1,0,1] neg_lo:[1,0,0]\n"
    "v_cvt_pk_bf16_f32 v16, v216, v206\n"
    "v_cvt_pk_bf16_f32 v140, -v217, -v207\n"
    "v_pk_mul_f32 v[230:231], v[206:207], v[238:239] op_sel:[0,0] op_sel_hi:[0,1]\n"
    "v_pk_fma_f32 v[216:217], v[206:207], v[238:239], v[230:231] op_sel:[1,1,0] op_sel_hi:[1,0,1] neg_lo:[1,0,0]\n"
    "v_pk_mul_f32 v[230:231], v[216:217], v[238:239] op_sel:[0,0] op_sel_hi:[0,1]\n"
    "v_pk_fma_f32 v[206:207], v[216:217], v[238:239], v[230:231] op_sel:[1,1,0] op_sel_hi:[1,0,1] neg_lo:[1,0,0]\n"
    "buffer_load_dwordx4 v10, s[24:27], s64 offen lds\n"
    "v_cvt_pk_bf16_f32 v17, v216, v206\n"
    "v_cvt_pk_bf16_f32 v141, -v217, -v207\n"
    "v_pk_mul_f32 v[230:231], v[206:207], v[238:239] op_sel:[0,0] op_sel_hi:[0,1]\n"
    "v_pk_fma_f32 v[216:217], v[206:207], v[238:239], v[230:231] op_sel:[1,1,0] op_sel_hi:[1,0,1] neg_lo:[1,0,0]\n"
    "v_pk_mul_f32 v[230:231], v[216:217], v[238:239] op_sel:[0,0] op_sel_hi:[0,1]\n"
    "v_pk_fma_f32 v[206:207], v[216:217], v[238:239], v[230:231] op_sel:[1,1,0] op_sel_hi:[1,0,1] neg_lo:[1,0,0]\n"
    "v_cvt_pk_bf16_f32 v18, v216, v206\n"
    "v_cvt_pk_bf16_f32 v142, -v217, -v207\n"
    "v_pk_mul_f32 v[230:231], v[206:207], v[238:239] op_sel:[0,0] op_sel_hi:[0,1]\n"
    "v_pk_fma_f32 v[216:217], v[206:207], v[238:239], v[230:231] op_sel:[1,1,0] op_sel_hi:[1,0,1] neg_lo:[1,0,0]\n"
    "v_pk_mul_f32 v[230:231], v[216:217], v[238:239] op_sel:[0,0] op_sel_hi:[0,1]\n"
    "v_pk_fma_f32 v[206:207], v[216:217], v[238:239], v[230:231] op_sel:[1,1,0] op_sel_hi:[1,0,1] neg_lo:[1,0,0]\n"
    "v_cvt_pk_bf16_f32 v19, v216, v206\n"
    "v_cvt_pk_bf16_f32 v143, -v217, -v207\n"
    "v_pk_mul_f32 v[230:231], v[206:207], v[238:239] op_sel:[0,0] op_sel_hi:[0,1]\n"
    "v_pk_fma_f32 v[216:217], v[206:207], v[238:239], v[230:231] op_sel:[1,1,0] op_sel_hi:[1,0,1] neg_lo:[1,0,0]\n"
    "v_pk_mul_f32 v[230:231], v[216:217], v[238:239] op_sel:[0,0] op_sel_hi:[0,1]\n"
    "v_pk_fma_f32 v[206:207], v[216:217], v[238:239], v[230:231] op_sel:[1,1,0] op_sel_hi:[1,0,1] neg_lo:[1,0,0]\n"
    "v_cvt_pk_bf16_f32 v20, v216, v206\n"
    "v_cvt_pk_bf16_f32 v144, -v217, -v207\n"
    "buffer_load_dwordx4 v11, s[24:27], s64 offen offset:1024 lds\n"
    "v_pk_mul_f32 v[230:231], v[206:207], v[238:239] op_sel:[0,0] op_sel_hi:[0,1]\n"
    "v_pk_fma_f32 v[216:217], v[206:207], v[238:239], v[230:231] op_sel:[1,1,0] op_sel_hi:[1,0,1] neg_lo:[1,0,0]\n"
    "v_pk_mul_f32 v[230:231], v[216:217], v[238:239] op_sel:[0,0] op_sel_hi:[0,1]\n"
    "v_pk_fma_f32 v[206:207], v[216:217], v[238:239], v[230:231] op_sel:[1,1,0] op_sel_hi:[1,0,1] neg_lo:[1,0,0]\n"
    "v_cvt_pk_bf16_f32 v21, v216, v206\n"
    "v_cvt_pk_bf16_f32 v145, -v217, -v207\n"
    "ds_write_b128 v211, v[14:17]\n"
    "s_waitcnt vmcnt(7)\n"
    "buffer_store_dwordx2 v[222:223], v227, s[36:39], s21 offen sc1\n"
    "ds_write_b128 v211, v[18:21] offset:16\n"
    "v_pk_mul_f32 v[230:231], v[240:241], v[240:241] op_sel:[0,0] op_sel_hi:[0,1]\n"
    "v_pk_fma_f32 v[162:163], v[240:241], v[240:241], v[230:231] op_sel:[1,1,0] op_sel_hi:[1,0,1] neg_lo:[1,0,0]\n"
    "v_pk_mul_f32 v[230:231], v[162:163], v[240:241] op_sel:[0,0] op_sel_hi:[0,1]\n"
    "v_pk_fma_f32 v[164:165], v[162:163], v[240:241], v[230:231] op_sel:[1,1,0] op_sel_hi:[1,0,1] neg_lo:[1,0,0]\n"
    "buffer_store_dwordx4 v[66:69], v212, s[36:39], s91 offen sc1\n"
    "s_add_u32 s91, s91, 0x8000\n"
    "s_waitcnt vmcnt(8)\n"
    "s_barrier\n"
    "ds_read_b64_tr_b16 a[0:1], v221\n"
    "ds_read_b64_tr_b16 a[64:65], v220\n"
    "ds_read_b64_tr_b16 a[128:129], v220 offset:1024\n"
    "ds_read_b64_tr_b16 a[2:3], v221 offset:6144\n"
    "ds_read_b64_tr_b16 a[66:67], v220 offset:6144\n"
    "ds_read_b64_tr_b16 a[130:131], v220 offset:7168\n"
    "ds_read_b64_tr_b16 a[4:5], v221 offset:12288\n"
    "ds_read_b64_tr_b16 a[68:69], v220 offset:12288\n"
    "buffer_store_dwordx4 v[70:73], v212, s[36:39], s91 offen sc1\n"
    "s_lshl2_add_u32 s44, s6, s3\n"
    "s_lshl_b32 s44, s44, 5\n"
    "s_add_u32 s44, s44, s8\n"
    "s_lshl3_add_u32 s22, s44, 0x5000\n"
    "s_lshr_b32 s44, s8, 3\n"
    "s_lshl2_add_u32 s44, s6, s44\n"
    "s_lshl_b32 s44, s44, 8\n"
    "s_add_u32 s23, s44, 0x5000\n"
    "s_lshl2_add_u32 s19, s8, s3\n"
    "s_lshl_b32 s19, s19, 14\n"
    "s_add_u32 s19, s19, 0x40000\n"
    "ds_read_b64_tr_b16 a[132:133], v220 offset:13312\n"
    "ds_read_b64_tr_b16 a[6:7], v221 offset:18432\n"
    "ds_read_b64_tr_b16 a[70:71], v220 offset:18432\n"
    "ds_read_b64_tr_b16 a[134:135], v220 offset:19456\n"
    "v_pk_mul_f32 v[230:231], v[164:165], v[240:241] op_sel:[0,0] op_sel_hi:[0,1]\n"
    "v_pk_fma_f32 v[166:167], v[164:165], v[240:241], v[230:231] op_sel:[1,1,0] op_sel_hi:[1,0,1] neg_lo:[1,0,0]\n"
    "v_pk_mul_f32 v[230:231], v[166:167], v[240:241] op_sel:[0,0] op_sel_hi:[0,1]\n"
    "v_pk_fma_f32 v[168:169], v[166:167], v[240:241], v[230:231] op_sel:[1,1,0] op_sel_hi:[1,0,1] neg_lo:[1,0,0]\n"
    "v_pk_mul_f32 v[230:231], v[168:169], v[240:241] op_sel:[0,0] op_sel_hi:[0,1]\n"
    "buffer_load_dwordx4 v12, s[24:27], s64 offen offset:2048 lds\n"
    "ds_write_b128 v211, v[138:141] offset:32\n"
    "v_pk_fma_f32 v[170:171], v[168:169], v[240:241], v[230:231] op_sel:[1,1,0] op_sel_hi:[1,0,1] neg_lo:[1,0,0]\n"
    "v_pk_mul_f32 v[230:231], v[170:171], v[240:241] op_sel:[0,0] op_sel_hi:[0,1]\n"
    "v_pk_fma_f32 v[172:173], v[170:171], v[240:241], v[230:231] op_sel:[1,1,0] op_sel_hi:[1,0,1] neg_lo:[1,0,0]\n"
    "v_cvt_pk_bf16_f32 v146, 1.0, v240\n"
    "v_cvt_pk_bf16_f32 v154, 0, v241\n"
    "v_cvt_pk_bf16_f32 v147, v162, v164\n"
    "ds_write_b128 v211, v[142:145] offset:48\n"
    "buffer_load_dwordx4 v13, s[24:27], s64 offen offset:3072 lds\n"
    "v_cvt_pk_bf16_f32 v155, v163, v165\n"
    "v_cvt_pk_bf16_f32 v148, v166, v168\n"
    "v_cvt_pk_bf16_f32 v156, v167, v169\n"
    "v_cvt_pk_bf16_f32 v149, v170, v172\n"
    "ds_read_b64_tr_b16 a[192:193], v210\n"
    "ds_read_b64_tr_b16 a[194:195], v210 offset:80\n"
    "ds_read_b64_tr_b16 a[196:197], v210 offset:1280\n"
    "ds_read_b64_tr_b16 a[198:199], v210 offset:1360\n"
    "ds_read_b64_tr_b16 a[200:201], v210 offset:2560\n"
    "buffer_store_dwordx4 v[30:33], v252, s[36:39], s82 offen sc1\n"
    "s_add_u32 s64, s63, 0x2b000\n"
    "s_add_u32 m0, s81, 0x6000\n"
    "ds_read_b64_tr_b16 a[202:203], v210 offset:2640\n"
    "ds_read_b64_tr_b16 a[204:205], v210 offset:3840\n"
    "ds_read_b64_tr_b16 a[206:207], v210 offset:3920\n"
    "v_cvt_pk_bf16_f32 v157, v171, v173\n"
    "v_pk_mul_f32 v[230:231], v[240:241], v[236:237] op_sel:[0,0] op_sel_hi:[0,1]\n"
    "v_pk_fma_f32 v[206:207], v[240:241], v[236:237], v[230:231] op_sel:[1,1,0] op_sel_hi:[1,0,1] neg_lo:[1,0,0]\n"
    "v_cvt_pk_bf16_f32 v150, v236, v206\n"
    "v_cvt_pk_bf16_f32 v158, v237, v207\n"
    "v_pk_mul_f32 v[230:231], v[162:163], v[236:237] op_sel:[0,0] op_sel_hi:[0,1]\n"
    "v_pk_fma_f32 v[216:217], v[162:163], v[236:237], v[230:231] op_sel:[1,1,0] op_sel_hi:[1,0,1] neg_lo:[1,0,0]\n"
    "v_pk_mul_f32 v[230:231], v[164:165], v[236:237] op_sel:[0,0] op_sel_hi:[0,1]\n"
    "v_pk_fma_f32 v[206:207], v[164:165], v[236:237], v[230:231] op_sel:[1,1,0] op_sel_hi:[1,0,1] neg_lo:[1,0,0]\n"
    "v_cvt_pk_bf16_f32 v151, v216, v206\n"
    "v_cvt_pk_bf16_f32 v159, v217, v207\n"
    "v_pk_mul_f32 v[230:231], v[166:167], v[236:237] op_sel:[0,0] op_sel_hi:[0,1]\n"
    "v_pk_fma_f32 v[216:217], v[166:167], v[236:237], v[230:231] op_sel:[1,1,0] op_sel_hi:[1,0,1] neg_lo:[1,0,0]\n"
    "v_pk_mul_f32 v[230:231], v[168:169], v[236:237] op_sel:[0,0] op_sel_hi:[0,1]\n"
    "v_pk_fma_f32 v[206:207], v[168:169], v[236:237], v[230:231] op_sel:[1,1,0] op_sel_hi:[1,0,1] neg_lo:[1,0,0]\n"
    "v_cvt_pk_bf16_f32 v152, v216, v206\n"
    "buffer_load_dwordx4 v224, s[28:31], s64 offen lds\n"
    "v_cvt_pk_bf16_f32 v160, v217, v207\n"
    "v_pk_mul_f32 v[230:231], v[170:171], v[236:237] op_sel:[0,0] op_sel_hi:[0,1]\n"
    "v_pk_fma_f32 v[216:217], v[170:171], v[236:237], v[230:231] op_sel:[1,1,0] op_sel_hi:[1,0,1] neg_lo:[1,0,0]\n"
    "v_pk_mul_f32 v[230:231], v[172:173], v[236:237] op_sel:[0,0] op_sel_hi:[0,1]\n"
    "v_pk_fma_f32 v[206:207], v[172:173], v[236:237], v[230:231] op_sel:[1,1,0] op_sel_hi:[1,0,1] neg_lo:[1,0,0]\n"
    "v_cvt_pk_bf16_f32 v153, v216, v206\n"
    "v_cvt_pk_bf16_f32 v161, v217, v207\n"
    "ds_write_b128 v211, v[146:149]\n"
    "v_pk_mul_f32 v[230:231], v[236:237], v[236:237] op_sel:[0,0] op_sel_hi:[0,1]\n"
    "v_pk_fma_f32 v[238:239], v[236:237], v[236:237], v[230:231] op_sel:[1,1,0] op_sel_hi:[1,0,1] neg_lo:[1,0,0]\n"
    "v_pk_mul_f32 v[230:231], v[238:239], v[162:163] op_sel:[0,0] op_sel_hi:[0,1]\n"
    "v_pk_fma_f32 v[216:217], v[238:239], v[162:163], v[230:231] op_sel:[1,1,0] op_sel_hi:[1,0,1] neg_lo:[1,0,0]\n"
    "v_cvt_pk_bf16_f32 v14, v238, v216\n"
    "v_cvt_pk_bf16_f32 v138, v239, v217\n"
    "buffer_load_dwordx4 v225, s[28:31], s64 offen offset:1024 lds\n"
    "ds_write_b128 v211, v[150:153] offset:16\n"
    "v_pk_mul_f32 v[230:231], v[238:239], v[166:167] op_sel:[0,0] op_sel_hi:[0,1]\n"
    "v_pk_fma_f32 v[216:217], v[238:239], v[166:167], v[230:231] op_sel:[1,1,0] op_sel_hi:[1,0,1] neg_lo:[1,0,0]\n"
    "v_pk_mul_f32 v[230:231], v[238:239], v[170:171] op_sel:[0,0] op_sel_hi:[0,1]\n"
    "v_pk_fma_f32 v[206:207], v[238:239], v[170:171], v[230:231] op_sel:[1,1,0] op_sel_hi:[1,0,1] neg_lo:[1,0,0]\n"
    "v_cvt_pk_bf16_f32 v15, v216, v206\n"
    "v_cvt_pk_bf16_f32 v139, v217, v207\n"
    "ds_write_b128 v211, v[154:157] offset:32\n"
    "buffer_load_dwordx4 v226, s[28:31], s64 offen offset:2048 lds\n"
    "v_pk_mul_f32 v[230:231], v[238:239], v[236:237] op_sel:[0,0] op_sel_hi:[0,1]\n"
    "v_pk_fma_f32 v[240:241], v[238:239], v[236:237], v[230:231] op_sel:[1,1,0] op_sel_hi:[1,0,1] neg_lo:[1,0,0]\n"
    "v_pk_mul_f32 v[230:231], v[240:241], v[162:163] op_sel:[0,0] op_sel_hi:[0,1]\n"
    "v_pk_fma_f32 v[216:217], v[240:241], v[162:163], v[230:231] op_sel:[1,1,0] op_sel_hi:[1,0,1] neg_lo:[1,0,0]\n"
    "ds_write_b128 v211, v[158:161] offset:48\n"
    "v_cvt_pk_bf16_f32 v16, v240, v216\n"
    "v_cvt_pk_bf16_f32 v140, v241, v217\n"
    "v_pk_mul_f32 v[230:231], v[240:241], v[166:167] op_sel:[0,0] op_sel_hi:[0,1]\n"
    "buffer_store_dwordx2 v[30:31], v232, s[36:39], s83 offen sc1\n"
    "s_add_u32 s64, s63, 0x40800\n"
    "s_add_u32 m0, s81, 0x9000\n"
    "ds_read_b64_tr_b16 a[208:209], v210\n"
    "ds_read_b64_tr_b16 a[210:211], v210 offset:80\n"
    "ds_read_b64_tr_b16 a[212:213], v210 offset:1280\n"
    "ds_read_b64_tr_b16 a[214:215], v210 offset:1360\n"
    "ds_read_b64_tr_b16 a[216:217], v210 offset:2560\n"
    "buffer_load_dwordx4 v224, s[28:31], s64 offen lds\n"
    "ds_read_b64_tr_b16 a[218:219], v210 offset:2640\n"
    "ds_read_b64_tr_b16 a[220:221], v210 offset:3840\n"
    "ds_read_b64_tr_b16 a[222:223], v210 offset:3920\n"
    "v_pk_fma_f32 v[216:217], v[240:241], v[166:167], v[230:231] op_sel:[1,1,0] op_sel_hi:[1,0,1] neg_lo:[1,0,0]\n"
    "v_pk_mul_f32 v[230:231], v[240:241], v[170:171] op_sel:[0,0] op_sel_hi:[0,1]\n"
    "v_pk_fma_f32 v[206:207], v[240:241], v[170:171], v[230:231] op_sel:[1,1,0] op_sel_hi:[1,0,1] neg_lo:[1,0,0]\n"
    "v_cvt_pk_bf16_f32 v17, v216, v206\n"
    "v_cvt_pk_bf16_f32 v141, v217, v207\n"
    "v_pk_mul_f32 v[230:231], v[240:241], v[236:237] op_sel:[0,0] op_sel_hi:[0,1]\n"
    "v_pk_fma_f32 v[238:239], v[240:241], v[236:237], v[230:231] op_sel:[1,1,0] op_sel_hi:[1,0,1] neg_lo:[1,0,0]\n"
    "v_pk_mul_f32 v[230:231], v[238:239], v[166:167] op_sel:[0,0] op_sel_hi:[0,1]\n"
    "v_pk_fma_f32 v[216:217], v[238:239], v[166:167], v[230:231] op_sel:[1,1,0] op_sel_hi:[1,0,1] neg_lo:[1,0,0]\n"
    "v_cvt_pk_bf16_f32 v18, v238, v216\n"
    "v_cvt_pk_bf16_f32 v142, v239, v217\n"
    "v_pk_mul_f32 v[230:231], v[238:239], v[236:237] op_sel:[0,0] op_sel_hi:[0,1]\n"
    "v_pk_fma_f32 v[240:241], v[238:239], v[236:237], v[230:231] op_sel:[1,1,0] op_sel_hi:[1,0,1] neg_lo:[1,0,0]\n"
    "v_pk_mul_f32 v[230:231], v[240:241], v[166:167] op_sel:[0,0] op_sel_hi:[0,1]\n"
    "v_pk_fma_f32 v[216:217], v[240:241], v[166:167], v[230:231] op_sel:[1,1,0] op_sel_hi:[1,0,1] neg_lo:[1,0,0]\n"
    "buffer_load_dwordx4 v225, s[28:31], s64 offen offset:1024 lds\n"
    "v_cvt_pk_bf16_f32 v19, v240, v216\n"
    "v_cvt_pk_bf16_f32 v143, v241, v217\n"
    "v_pk_mul_f32 v[230:231], v[240:241], v[236:237] op_sel:[0,0] op_sel_hi:[0,1]\n"
    "v_pk_fma_f32 v[238:239], v[240:241], v[236:237], v[230:231] op_sel:[1,1,0] op_sel_hi:[1,0,1] neg_lo:[1,0,0]\n"
    "v_pk_mul_f32 v[230:231], v[238:239], v[166:167] op_sel:[0,0] op_sel_hi:[0,1]\n"
    "v_pk_fma_f32 v[216:217], v[238:239], v[166:167], v[230:231] op_sel:[1,1,0] op_sel_hi:[1,0,1] neg_lo:[1,0,0]\n"
    "v_cvt_pk_bf16_f32 v20, v238, v216\n"
    "v_cvt_pk_bf16_f32 v144, v239, v217\n"
    "v_pk_mul_f32 v[230:231], v[238:239], v[236:237] op_sel:[0,0] op_sel_hi:[0,1]\n"
    "v_pk_fma_f32 v[240:241], v[238:239], v[236:237], v[230:231] op_sel:[1,1,0] op_sel_hi:[1,0,1] neg_lo:[1,0,0]\n"
    "v_pk_mul_f32 v[230:231], v[240:241], v[166:167] op_sel:[0,0] op_sel_hi:[0,1]\n"
    "v_pk_fma_f32 v[216:217], v[240:241], v[166:167], v[230:231] op_sel:[1,1,0] op_sel_hi:[1,0,1] neg_lo:[1,0,0]\n"
    "v_cvt_pk_bf16_f32 v21, v240, v216\n"
    "v_cvt_pk_bf16_f32 v145, v241, v217\n"
    "v_pk_mul_f32 v[230:231], v[240:241], v[236:237] op_sel:[0,0] op_sel_hi:[0,1]\n"
    "v_pk_fma_f32 v[238:239], v[240:241], v[236:237], v[230:231] op_sel:[1,1,0] op_sel_hi:[1,0,1] neg_lo:[1,0,0]\n"
    "v_pk_mul_f32 v[230:231], v[238:239], v[236:237] op_sel:[0,0] op_sel_hi:[0,1]\n"
    "v_pk_fma_f32 v[240:241], v[238:239], v[236:237], v[230:231] op_sel:[1,1,0] op_sel_hi:[1,0,1] neg_lo:[1,0,0]\n"
    "v_cvt_pk_bf16_f32 v146, v238, v240\n"
    "v_cvt_pk_bf16_f32 v154, v239, v241\n"
    "buffer_load_dwordx4 v226, s[28:31], s64 offen offset:2048 lds\n"
    "s_add_u32 s65, s82, 0xac00\n"
    "ds_write_b128 v211, v[14:17]\n"
    "v_pk_mul_f32 v[230:231], v[240:241], v[236:237] op_sel:[0,0] op_sel_hi:[0,1]\n"
    "v_pk_fma_f32 v[238:239], v[240:241], v[236:237], v[230:231] op_sel:[1,1,0] op_sel_hi:[1,0,1] neg_lo:[1,0,0]\n"
    "v_pk_mul_f32 v[230:231], v[238:239], v[236:237] op_sel:[0,0] op_sel_hi:[0,1]\n"
    "v_pk_fma_f32 v[240:241], v[238:239], v[236:237], v[230:231] op_sel:[1,1,0] op_sel_hi:[1,0,1] neg_lo:[1,0,0]\n"
    "v_cvt_pk_bf16_f32 v147, v238, v240\n"
    "v_cvt_pk_bf16_f32 v155, v239, v241\n"
    "buffer_store_dwordx4 v[30:33], v252, s[36:39], s65 offen sc1\n"
    "s_add_u32 s16, s12, 1\n"
    "s_and_b32 s16, s16, 3\n"
    "s_cselect_b32 s16, 1, -3\n"
    "s_mul_i32 s16, s16, 0x56000\n"
    "s_add_u32 s54, s63, s16\n"
    "s_add_u32 m0, s81, 0xc000\n"
    "ds_write_b128 v211, v[18:21] offset:16\n"
    "v_pk_mul_f32 v[230:231], v[240:241], v[236:237] op_sel:[0,0] op_sel_hi:[0,1]\n"
    "v_pk_fma_f32 v[238:239], v[240:241], v[236:237], v[230:231] op_sel:[1,1,0] op_sel_hi:[1,0,1] neg_lo:[1,0,0]\n"
    "v_pk_mul_f32 v[230:231], v[238:239], v[236:237] op_sel:[0,0] op_sel_hi:[0,1]\n"
    "v_pk_fma_f32 v[240:241], v[238:239], v[236:237], v[230:231] op_sel:[1,1,0] op_sel_hi:[1,0,1] neg_lo:[1,0,0]\n"
    "v_cvt_pk_bf16_f32 v148, v238, v240\n"
    "v_cvt_pk_bf16_f32 v156, v239, v241\n"
    "v_pk_mul_f32 v[230:231], v[240:241], v[236:237] op_sel:[0,0] op_sel_hi:[0,1]\n"
    "v_pk_fma_f32 v[238:239], v[240:241], v[236:237], v[230:231] op_sel:[1,1,0] op_sel_hi:[1,0,1] neg_lo:[1,0,0]\n"
    "v_pk_mul_f32 v[230:231], v[238:239], v[236:237] op_sel:[0,0] op_sel_hi:[0,1]\n"
    "v_pk_fma_f32 v[240:241], v[238:239], v[236:237], v[230:231] op_sel:[1,1,0] op_sel_hi:[1,0,1] neg_lo:[1,0,0]\n"
    "v_cvt_pk_bf16_f32 v149, v238, v240\n"
    "v_cvt_pk_bf16_f32 v157, v239, v241\n"
    "buffer_load_dwordx4 v224, s[28:31], s54 offen lds\n"
    "ds_write_b128 v211, v[138:141] offset:32\n"
    "v_pk_mul_f32 v[230:231], v[240:241], v[236:237] op_sel:[0,0] op_sel_hi:[0,1]\n"
    "v_pk_fma_f32 v[238:239], v[240:241], v[236:237], v[230:231] op_sel:[1,1,0] op_sel_hi:[1,0,1] neg_lo:[1,0,0]\n"
    "v_cvt_pk_bf16_f32 v150, v238, v238\n"
    "v_cvt_pk_bf16_f32 v158, v239, v239\n"
    "s_waitcnt lgkmcnt(2)\n"
    "v_mfma_f32_16x16x32_bf16 v[26:29], a[0:3], v[22:25], 0\n"
    "ds_write_b128 v211, v[142:145] offset:48\n"
    "buffer_load_dwordx4 v225, s[28:31], s54 offen offset:1024 lds\n"
    "v_mfma_f32_16x16x32_bf16 v[34:37], a[64:67], v[22:25], 0\n"
    "v_mfma_f32_16x16x32_bf16 v[114:117], a[128:131], v[22:25], 0\n"
    "ds_read_b64_tr_b16 a[224:225], v210\n"
    "ds_read_b64_tr_b16 a[226:227], v210 offset:80\n"
    "ds_read_b64_tr_b16 a[228:229], v210 offset:1280\n"
    "ds_read_b64_tr_b16 a[230:231], v210 offset:1360\n"
    "ds_read_b64_tr_b16 a[232:233], v210 offset:2560\n"
    "buffer_load_dwordx4 v226, s[28:31], s54 offen offset:2048 lds\n"
    "s_add_u32 s65, s83, 0xac00\n"
    "ds_read_b64_tr_b16 a[234:235], v210 offset:2640\n"
    "ds_read_b64_tr_b16 a[236:237], v210 offset:3840\n"
    "ds_read_b64_tr_b16 a[238:239], v210 offset:3920\n"
    "ds_write_b128 v211, v[146:149]\n"
    "v_mfma_f32_16x16x32_bf16 v[26:29], a[4:7], v[22:25], v[26:29]\n"
    "buffer_store_dwordx2 v[30:31], v232, s[36:39], s65 offen sc1\n"
    "s_add_u32 s16, s12, 2\n"
    "s_and_b32 s16, s16, 3\n"
    "s_cselect_b32 s16, 1, -3\n"
    "s_mul_i32 s16, s16, 0x56000\n"
    "s_add_u32 s55, s54, s16\n"
    "s_add_u32 s16, s12, 3\n"
    "s_and_b32 s16, s16, 3\n"
    "s_cselect_b32 s16, 1, -3\n"
    "s_mul_i32 s16, s16, 0x56000\n"
    "s_add_u32 s56, s55, s16\n"
    "s_lshr_b32 s16, %1, 3\n"
    "s_lshl2_add_u32 s16, s16, s3\n"
    "s_mul_i32 s16, s16, 0x2c00\n"
    "v_min_u32 v254, 10, v255\n"
    "v_lshl_add_u32 v254, v254, 10, s16\n"
    "s_sub_u32 s16, 0x180, s32\n"
    "s_and_b32 s16, s16, 0x380\n"
    "buffer_load_dword v254, v254, s[32:35], s16 offen\n"
    "ds_write_b32 v211, v150 offset:16\n"
    "v_mfma_f32_16x16x32_bf16 v[34:37], a[68:71], v[22:25], v[34:37]\n"
    "ds_write_b128 v211, v[154:157] offset:32\n"
    "v_mfma_f32_16x16x32_bf16 v[114:117], a[132:135], v[22:25], v[114:117]\n"
    "ds_write_b32 v211, v158 offset:48\n"
    "ds_read_b64_tr_b16 a[240:241], v210\n"
    "ds_read_b64_tr_b16 a[242:243], v210 offset:80\n"
    "ds_read_b64_tr_b16 a[244:245], v210 offset:1280\n"
    "ds_read_b64_tr_b16 a[246:247], v210 offset:1360\n"
    "ds_read_b64_tr_b16 a[248:249], v210 offset:2560\n"
    "ds_read_b64_tr_b16 a[250:251], v210 offset:2640\n"
    "ds_read_b64_tr_b16 a[252:253], v210 offset:3840\n"
    "ds_read_b64_tr_b16 a[254:255], v210 offset:3920\n"
    "v_mfma_f32_16x16x32_bf16 v[162:165], a[208:211], a[192:195], 0\n"
    "s_waitcnt lgkmcnt(10)\n"
    "v_mfma_f32_16x16x32_bf16 v[166:169], a[224:227], a[192:195], 0\n"
    "s_waitcnt vmcnt(6)\n"
    "s_barrier\n"
    "ds_read_b64_tr_b16 a[8:9], v221 offset:24576\n"
    "s_add_u32 s16, s54, 0x15800\n"
    "ds_read_b64_tr_b16 a[72:73], v220 offset:24576\n"
    "s_add_u32 m0, s81, 0\n"
    "ds_read_b64_tr_b16 a[136:137], v220 offset:25600\n"
    "ds_read_b64_tr_b16 a[10:11], v221 offset:30720\n"
    "ds_read_b64_tr_b16 a[74:75], v220 offset:30720\n"
    "ds_read_b64_tr_b16 a[138:139], v220 offset:31744\n"
    "ds_read_b64_tr_b16 a[12:13], v221 offset:36864\n"
    "ds_read_b64_tr_b16 a[76:77], v220 offset:36864\n"
    "ds_read_b64_tr_b16 a[140:141], v220 offset:37888\n"
    "ds_read_b64_tr_b16 a[14:15], v221 offset:43008\n"
    "ds_read_b64_tr_b16 a[78:79], v220 offset:43008\n"
    "ds_read_b64_tr_b16 a[142:143], v220 offset:44032\n"
    "buffer_load_dwordx4 v224, s[28:31], s16 offen lds\n"
    "s_waitcnt lgkmcnt(12)\n"
    "v_mfma_f32_16x16x32_bf16 v[170:173], a[240:243], a[192:195], 0\n"
    "ds_write_b128 v201, v[54:57]\n"
    "v_mfma_f32_16x16x32_bf16 v[162:165], a[212:215], a[196:199], v[162:165]\n"
    "v_mfma_f32_16x16x32_bf16 v[166:169], a[228:231], a[196:199], v[166:169]\n"
    "buffer_load_dwordx4 v225, s[28:31], s16 offen offset:1024 lds\n"
    "v_mfma_f32_16x16x32_bf16 v[170:173], a[244:247], a[196:199], v[170:173]\n"
    "v_mfma_f32_16x16x32_bf16 v[162:165], a[216:219], a[200:203], v[162:165]\n"
    "v_mfma_f32_16x16x32_bf16 v[166:169], a[232:235], a[200:203], v[166:169]\n"
    "v_mfma_f32_16x16x32_bf16 v[170:173], a[248:251], a[200:203], v[170:173]\n"
    "buffer_load_dwordx4 v226, s[28:31], s16 offen offset:2048 lds\n"
    "s_add_u32 s65, s82, 0x15800\n"
    "v_mfma_f32_16x16x32_bf16 v[162:165], a[220:223], a[204:207], v[162:165]\n"
    "v_mfma_f32_16x16x32_bf16 v[166:169], a[236:239], a[204:207], v[166:169]\n"
    "v_mfma_f32_16x16x32_bf16 v[170:173], a[252:255], a[204:207], v[170:173]\n"
    "s_nop 5\n"
    "ds_write_b128 v205, v[162:165] offset:16\n"
    "buffer_store_dwordx4 v[30:33], v252, s[36:39], s65 offen sc1\n"
    "s_waitcnt vmcnt(6)\n"
    "s_waitcnt lgkmcnt(1)\n"
    "s_barrier\n"
    "ds_read_b64_tr_b16 a[16:17], v221 offset:49152\n"
    "s_add_u32 s16, s54, 0x2b000\n"
    "ds_read_b64_tr_b16 a[80:81], v220 offset:49152\n"
    "s_add_u32 m0, s81, 0x3000\n"
    "ds_read_b64_tr_b16 a[144:145], v220 offset:50176\n"
    "ds_read_b64_tr_b16 a[18:19], v221 offset:55296\n"
    "ds_read_b64_tr_b16 a[82:83], v220 offset:55296\n"
    "ds_read_b64_tr_b16 a[146:147], v220 offset:56320\n"
    "ds_write_b64 v204, v[166:167] offset:1040\n"
    "buffer_load_dwordx4 v224, s[28:31], s16 offen lds\n"
    "ds_write_b64 v203, v[168:169] offset:1040\n"
    "v_mfma_f32_16x16x32_bf16 v[26:29], a[8:11], v[22:25], v[26:29]\n"
    "ds_write_b32 v202, v170 offset:2064\n"
    "v_mfma_f32_16x16x32_bf16 v[34:37], a[72:75], v[22:25], v[34:37]\n"
    "ds_write_b32 v202, v171 offset:2128\n"
    "v_mfma_f32_16x16x32_bf16 v[114:117], a[136:139], v[22:25], v[114:117]\n"
    "ds_write_b32 v202, v172 offset:2192\n"
    "buffer_load_dwordx4 v225, s[28:31], s16 offen offset:1024 lds\n"
    "ds_write_b32 v202, v173 offset:2256\n"
    "v_mfma_f32_16x16x32_bf16 v[26:29], a[12:15], v[22:25], v[26:29]\n"
    "v_mfma_f32_16x16x32_bf16 v[34:37], a[76:79], v[22:25], v[34:37]\n"
    "v_mfma_f32_16x16x32_bf16 v[114:117], a[140:143], v[22:25], v[114:117]\n"
    "s_waitcnt lgkmcnt(5)\n"
    "v_mfma_f32_16x16x32_bf16 v[26:29], a[16:19], v[22:25], v[26:29]\n"
    "buffer_load_dwordx4 v226, s[28:31], s16 offen offset:2048 lds\n"
    "s_add_u32 s65, s83, 0x15800\n"
    "v_mfma_f32_16x16x32_bf16 v[34:37], a[80:83], v[22:25], v[34:37]\n"
    "v_mfma_f32_16x16x32_bf16 v[114:117], a[144:147], v[22:25], v[114:117]\n"
    "s_waitcnt lgkmcnt(0)\n"
    "s_barrier\n"
    "buffer_load_dwordx2 v[248:249], v229, s[36:39], s20 offen sc1\n"
    "s_lshl_b32 s14, s79, 12\n"
    "s_lshl_b32 s44, s10, 11\n"
    "s_add_u32 s14, s14, s44\n"
    "s_add_u32 s14, s14, 0x440000\n"
    "s_add_u32 s21, s44, s93\n"
    "ds_read_b64 v[14:15], v200\n"
    "ds_read_b32 v16, v199\n"
    "ds_read_b32 v17, v198 offset:1024\n"
    "ds_read_b32 v18, v197 offset:1024\n"
    "buffer_store_dwordx2 v[222:223], v227, s[36:39], s22 offen sc1\n"
    "ds_read_b32 v19, v196 offset:1024\n"
    "ds_read_b32 v20, v198 offset:1536\n"
    "ds_read_b32 v21, v198 offset:1540\n"
    "ds_read_b32 v138, v198 offset:2048\n"
    "ds_read_b32 v139, v198 offset:2052\n"
    "ds_read_b64 v[142:143], v200 offset:5120\n"
    "buffer_store_dwordx2 v[30:31], v232, s[36:39], s65 offen sc1\n"
    "s_add_u32 s16, s54, 0x40800\n"
    "s_add_u32 m0, s81, 0x6000\n"
    "ds_read_b32 v144, v199 offset:5120\n"
    "ds_read_b32 v145, v198 offset:6144\n"
    "ds_read_b32 v146, v197 offset:6144\n"
    "ds_read_b32 v147, v196 offset:6144\n"
    "ds_read_b32 v148, v198 offset:6656\n"
    "buffer_load_dwordx4 v224, s[28:31], s16 offen lds\n"
    "ds_read_b32 v149, v198 offset:6660\n"
    "ds_read_b32 v150, v198 offset:7168\n"
    "ds_read_b32 v151, v198 offset:7172\n"
    "s_waitcnt lgkmcnt(0)\n"
    "s_barrier\n"
    "buffer_load_dwordx4 v225, s[28:31], s16 offen offset:1024 lds\n"
    "v_pk_add_f32 v[14:15], v[14:15], v[142:143]\n"
    "v_pk_add_f32 v[16:17], v[16:17], v[144:145]\n"
    "v_pk_add_f32 v[18:19], v[18:19], v[146:147]\n"
    "v_pk_add_f32 v[20:21], v[20:21], v[148:149]\n"
    "v_pk_add_f32 v[138:139], v[138:139], v[150:151]\n"
    "v_cvt_pk_bf16_f32 v142, v15, v14\n"
    "v_cvt_pk_bf16_f32 v143, v14, v16\n"
    "v_cvt_pk_bf16_f32 v144, v18, v17\n"
    "v_cvt_pk_bf16_f32 v145, v17, v19\n"
    "v_cndmask_b32 v14, v20, v21, s[94:95]\n"
    "v_cndmask_b32 v15, v138, v139, s[94:95]\n"
    "v_cvt_pk_bf16_f32 v146, v21, v14\n"
    "v_cvt_pk_bf16_f32 v147, v20, v20\n"
    "v_cvt_pk_bf16_f32 v148, v14, v20\n"
    "v_mov_b32 v149, v147\n"
    "v_cvt_pk_bf16_f32 v150, v139, v139\n"
    "v_cvt_pk_bf16_f32 v151, v139, v15\n"
    "v_cvt_pk_bf16_f32 v152, v138, v138\n"
    "v_mov_b32 v153, v152\n"
    "v_mov_b32 v18, v150\n"
    "buffer_load_dwordx4 v226, s[28:31], s16 offen offset:2048 lds\n"
    "s_add_u32 s65, s82, 0x20400\n"
    "v_cvt_pk_bf16_f32 v19, v15, v138\n"
    "v_mov_b32 v20, v152\n"
    "v_mov_b32 v21, v152\n"
    "ds_write_b32 v195, v142 offset:4092\n"
    "ds_write_b32 v195, v143 offset:8252\n"
    "ds_write_b32 v195, v144 offset:3580\n"
    "buffer_store_dwordx4 v[30:33], v252, s[36:39], s65 offen sc1\n"
    "s_add_u32 m0, s81, 0x9000\n"
    "ds_write_b32 v195, v145 offset:7740\n"
    "ds_write_b64 v194, v[146:147] offset:3064\n"
    "ds_write_b64 v194, v[148:149] offset:7224\n"
    "buffer_load_dwordx4 v224, s[28:31], s55 offen lds\n"
    "ds_write_b128 v193, v[150:153] offset:2032\n"
    "ds_write_b128 v193, v[18:21] offset:6192\n"
    "buffer_load_dwordx4 v225, s[28:31], s55 offen offset:1024 lds\n"
    "ds_write_b128 v192, v[54:57] offset:4096\n"
    "buffer_load_dwordx4 v226, s[28:31], s55 offen offset:2048 lds\n"
    "s_add_u32 s65, s83, 0x20400\n"
    "ds_write_b128 v191, v[54:57]\n"
    "s_waitcnt vmcnt(10)\n"
    "s_barrier\n"
    "ds_read_b64_tr_b16 a[20:21], v221\n"
    "ds_read_b64_tr_b16 a[84:85], v220\n"
    "ds_read_b64_tr_b16 a[148:149], v220 offset:1024\n"
    "ds_read_b64_tr_b16 a[22:23], v221 offset:6144\n"
    "ds_read_b64_tr_b16 a[86:87], v220 offset:6144\n"
    "ds_read_b64_tr_b16 a[150:151], v220 offset:7168\n"
    "ds_read_b64_tr_b16 a[24:25], v221 offset:12288\n"
    "ds_read_b64_tr_b16 a[88:89], v220 offset:12288\n"
    "s_waitcnt vmcnt(9)\n"
    "v_cmp_ne_u64 vcc, s[88:89], v[248:249]\n"
    "s_nop 1\n"
    "s_cbranch_vccnz .Lspin_1_%=\n"
    ".Lback_2_%=:\n"
    "s_lshl_b32 s18, s68, 12\n"
    "s_add_u32 s18, s18, s19\n"
    "buffer_load_dwordx4 a[40:43], v252, s[36:39], s18 offen sc1\n"
    "ds_read_b64_tr_b16 a[152:153], v220 offset:13312\n"
    "ds_read_b64_tr_b16 a[26:27], v221 offset:18432\n"
    "ds_read_b64_tr_b16 a[90:91], v220 offset:18432\n"
    "ds_read_b64_tr_b16 a[154:155], v220 offset:19456\n"
    "ds_read_b64_tr_b16 v[14:15], v46\n"
    "ds_read_b64_tr_b16 v[16:17], v50\n"
    "ds_read_b64_tr_b16 v[18:19], v47\n"
    "ds_read_b64_tr_b16 v[20:21], v51\n"
    "buffer_load_dwordx4 a[104:107], v252, s[36:39], s18 offen offset:1024 sc1\n"
    "ds_read_b64_tr_b16 v[38:39], v48\n"
    "ds_read_b64_tr_b16 v[40:41], v52\n"
    "ds_read_b64_tr_b16 v[54:55], v49\n"
    "ds_read_b64_tr_b16 v[56:57], v53\n"
    "ds_read_b64_tr_b16 v[66:67], v46 offset:16384\n"
    "ds_read_b64_tr_b16 v[68:69], v50 offset:16384\n"
    "ds_read_b64_tr_b16 v[70:71], v47 offset:16384\n"
    "ds_read_b64_tr_b16 v[72:73], v51 offset:16384\n"
    "buffer_load_dwordx4 a[168:171], v252, s[36:39], s18 offen offset:2048 sc1\n"
    "ds_read_b64_tr_b16 v[74:75], v48 offset:16384\n"
    "ds_read_b64_tr_b16 v[76:77], v52 offset:16384\n"
    "ds_read_b64_tr_b16 v[78:79], v49 offset:16384\n"
    "ds_read_b64_tr_b16 v[80:81], v53 offset:16384\n"
    "s_waitcnt lgkmcnt(12)\n"
    "v_mfma_f32_16x16x32_bf16 v[26:29], a[20:23], v[22:25], v[26:29]\n"
    "v_mfma_f32_16x16x32_bf16 v[34:37], a[84:87], v[22:25], v[34:37]\n"
    "buffer_load_dwordx4 a[44:47], v252, s[36:39], s18 offen offset:3072 sc1\n"
    "s_lshl_b32 s18, s69, 12\n"
    "s_add_u32 s18, s18, s19\n"
    "v_mfma_f32_16x16x32_bf16 v[114:117], a[148:151], v[22:25], v[114:117]\n"
    "v_mfma_f32_16x16x32_bf16 v[26:29], a[24:27], v[22:25], v[26:29]\n"
    "v_mfma_f32_16x16x32_bf16 v[34:37], a[88:91], v[22:25], v[34:37]\n"
    "v_mfma_f32_16x16x32_bf16 v[114:117], a[152:155], v[22:25], v[114:117]\n"
    "buffer_load_dwordx4 a[108:111], v252, s[36:39], s18 offen sc1\n"
    "buffer_load_dwordx4 a[172:175], v252, s[36:39], s18 offen offset:1024 sc1\n"
    "buffer_load_dwordx4 a[48:51], v252, s[36:39], s18 offen offset:2048 sc1\n"
    "buffer_load_dwordx4 a[112:115], v252, s[36:39], s18 offen offset:3072 sc1\n"
    "s_lshl_b32 s18, s70, 12\n"
    "s_add_u32 s18, s18, s19\n"
    "s_waitcnt vmcnt(8)\n"
    "s_barrier\n"
    "buffer_load_dwordx4 a[176:179], v252, s[36:39], s18 offen sc1\n"
    "ds_read_b64_tr_b16 a[28:29], v221 offset:24576\n"
    "ds_read_b64_tr_b16 a[92:93], v220 offset:24576\n"
    "ds_read_b64_tr_b16 a[156:157], v220 offset:25600\n"
    "ds_read_b64_tr_b16 a[30:31], v221 offset:30720\n"
    "ds_read_b64_tr_b16 a[94:95], v220 offset:30720\n"
    "ds_read_b64_tr_b16 a[158:159], v220 offset:31744\n"
    "ds_read_b64_tr_b16 a[32:33], v221 offset:36864\n"
    "ds_read_b64_tr_b16 a[96:97], v220 offset:36864\n"
    "buffer_load_dwordx4 a[52:55], v252, s[36:39], s18 offen offset:1024 sc1\n"
    "ds_read_b64_tr_b16 a[160:161], v220 offset:37888\n"
    "ds_read_b64_tr_b16 a[34:35], v221 offset:43008\n"
    "ds_read_b64_tr_b16 a[98:99], v220 offset:43008\n"
    "ds_read_b64_tr_b16 a[162:163], v220 offset:44032\n"
    "s_waitcnt vmcnt(9)\n"
    "v_mfma_f32_16x16x32_bf16 v[90:93], v[14:17], a[40:43], v[90:93]\n"
    "ds_read_b64_tr_b16 v[82:83], v46 offset:32768\n"
    "buffer_load_dwordx4 a[116:119], v252, s[36:39], s18 offen offset:2048 sc1\n"
    "ds_read_b64_tr_b16 v[84:85], v50 offset:32768\n"
    "v_mfma_f32_16x16x32_bf16 v[94:97], v[18:21], a[40:43], v[94:97]\n"
    "ds_read_b64_tr_b16 v[86:87], v47 offset:32768\n"
    "ds_read_b64_tr_b16 v[88:89], v51 offset:32768\n"
    "s_waitcnt lgkmcnt(9)\n"
    "v_mfma_f32_16x16x32_bf16 v[98:101], v[38:41], a[40:43], v[98:101]\n"
    "ds_read_b64_tr_b16 v[122:123], v48 offset:32768\n"
    "ds_read_b64_tr_b16 v[124:125], v52 offset:32768\n"
    "v_mfma_f32_16x16x32_bf16 v[102:105], v[54:57], a[40:43], v[102:105]\n"
    "ds_read_b64_tr_b16 v[126:127], v49 offset:32768\n"
    "buffer_load_dwordx4 a[180:183], v252, s[36:39], s18 offen offset:3072 sc1\n"
    "s_lshl_b32 s18, s71, 12\n"
    "s_add_u32 s18, s18, s19\n"
    "s_waitcnt lgkmcnt(7)\n"
    "s_barrier\n"
    "ds_read_b64_tr_b16 v[128:129], v53 offset:32768\n"
    "s_waitcnt vmcnt(10)\n"
    "v_mfma_f32_16x16x32_bf16 v[90:93], v[66:69], a[104:107], v[90:93]\n"
    "ds_read_b64_tr_b16 v[14:15], v46 offset:49152\n"
    "ds_read_b64_tr_b16 v[16:17], v50 offset:49152\n"
    "v_mfma_f32_16x16x32_bf16 v[94:97], v[70:73], a[104:107], v[94:97]\n"
    "ds_read_b64_tr_b16 v[18:19], v47 offset:49152\n"
    "ds_read_b64_tr_b16 v[20:21], v51 offset:49152\n"
    "buffer_load_dwordx4 a[56:59], v252, s[36:39], s18 offen sc1\n"
    "v_mfma_f32_16x16x32_bf16 v[98:101], v[74:77], a[104:107], v[98:101]\n"
    "ds_read_b64_tr_b16 v[38:39], v48 offset:49152\n"
    "ds_read_b64_tr_b16 v[40:41], v52 offset:49152\n"
    "v_mfma_f32_16x16x32_bf16 v[102:105], v[78:81], a[104:107], v[102:105]\n"
    "ds_read_b64_tr_b16 v[54:55], v49 offset:49152\n"
    "ds_read_b64_tr_b16 v[56:57], v53 offset:49152\n"
    "s_waitcnt vmcnt(10) lgkmcnt(9)\n"
    "v_mfma_f32_16x16x32_bf16 v[90:93], v[82:85], a[168:171], v[90:93]\n"
    "ds_read_b64_tr_b16 v[66:67], v46 offset:4096\n"
    "ds_read_b64_tr_b16 v[68:69], v50 offset:4096\n"
    "buffer_load_dwordx4 a[120:123], v252, s[36:39], s18 offen offset:1024 sc1\n"
    "v_mfma_f32_16x16x32_bf16 v[94:97], v[86:89], a[168:171], v[94:97]\n"
    "ds_read_b64_tr_b16 v[70:71], v47 offset:4096\n"
    "ds_read_b64_tr_b16 v[72:73], v51 offset:4096\n"
    "v_mfma_f32_16x16x32_bf16 v[98:101], v[122:125], a[168:171], v[98:101]\n"
    "ds_read_b64_tr_b16 v[74:75], v48 offset:4096\n"
    "ds_read_b64_tr_b16 v[76:77], v52 offset:4096\n"
    "s_waitcnt lgkmcnt(10)\n"
    "v_mfma_f32_16x16x32_bf16 v[102:105], v[126:129], a[168:171], v[102:105]\n"
    "ds_read_b64_tr_b16 v[78:79], v49 offset:4096\n"
    "ds_read_b64_tr_b16 v[80:81], v53 offset:4096\n"
    "buffer_load_dwordx2 v[246:247], v228, s[36:39], s23 offen sc1\n"
    "s_waitcnt vmcnt(11)\n"
    "v_mfma_f32_16x16x32_bf16 v[90:93], v[14:17], a[44:47], v[90:93]\n"
    "ds_read_b64_tr_b16 v[82:83], v46 offset:20480\n"
    "ds_read_b64_tr_b16 v[84:85], v50 offset:20480\n"
    "v_mfma_f32_16x16x32_bf16 v[94:97], v[18:21], a[44:47], v[94:97]\n"
    "ds_read_b64_tr_b16 v[86:87], v47 offset:20480\n"
    "ds_read_b64_tr_b16 v[88:89], v51 offset:20480\n"
    "s_waitcnt lgkmcnt(10)\n"
    "v_mfma_f32_16x16x32_bf16 v[98:101], v[38:41], a[44:47], v[98:101]\n"
    "ds_read_b64_tr_b16 v[122:123], v48 offset:20480\n"
    "buffer_load_dwordx4 a[184:187], v252, s[36:39], s18 offen offset:2048 sc1\n"
    "ds_read_b64_tr_b16 v[124:125], v52 offset:20480\n"
    "v_mfma_f32_16x16x32_bf16 v[102:105], v[54:57], a[44:47], v[102:105]\n"
    "ds_read_b64_tr_b16 v[126:127], v49 offset:20480\n"
    "ds_read_b64_tr_b16 v[128:129], v53 offset:20480\n"
    "s_waitcnt vmcnt(11)\n"
    "v_mfma_f32_16x16x32_bf16 v[90:93], v[66:69], a[108:111], v[90:93]\n"
    "ds_read_b64_tr_b16 v[14:15], v46 offset:36864\n"
    "ds_read_b64_tr_b16 v[16:17], v50 offset:36864\n"
    "s_waitcnt lgkmcnt(10)\n"
    "v_mfma_f32_16x16x32_bf16 v[94:97], v[70:73], a[108:111], v[94:97]\n"
    "ds_read_b64_tr_b16 v[18:19], v47 offset:36864\n"
    "buffer_load_dwordx4 v[118:121], v252, s[36:39], s18 offen offset:3072 sc1\n"
    "ds_read_b64_tr_b16 v[20:21], v51 offset:36864\n"
    "v_mfma_f32_16x16x32_bf16 v[98:101], v[74:77], a[108:111], v[98:101]\n"
    "ds_read_b64_tr_b16 v[38:39], v48 offset:36864\n"
    "ds_read_b64_tr_b16 v[40:41], v52 offset:36864\n"
    "v_mfma_f32_16x16x32_bf16 v[102:105], v[78:81], a[108:111], v[102:105]\n"
    "ds_read_b64_tr_b16 v[54:55], v49 offset:36864\n"
    "ds_read_b64_tr_b16 v[56:57], v53 offset:36864\n"
    "s_waitcnt vmcnt(11) lgkmcnt(10)\n"
    "v_mfma_f32_16x16x32_bf16 v[90:93], v[82:85], a[172:175], v[90:93]\n"
    "ds_read_b64_tr_b16 v[66:67], v46 offset:53248\n"
    "buffer_store_dwordx2 v[30:31], v232, s[36:39], s65 offen sc1\n"
    "s_add_u32 s16, s55, 0x15800\n"
    "s_add_u32 m0, s81, 0xc000\n"
    "ds_read_b64_tr_b16 v[68:69], v50 offset:53248\n"
    "v_mfma_f32_16x16x32_bf16 v[94:97], v[86:89], a[172:175], v[94:97]\n"
    "ds_read_b64_tr_b16 v[70:71], v47 offset:53248\n"
    "ds_read_b64_tr_b16 v[72:73], v51 offset:53248\n"
    "v_mfma_f32_16x16x32_bf16 v[98:101], v[122:125], a[172:175], v[98:101]\n"
    "ds_read_b64_tr_b16 v[74:75], v48 offset:53248\n"
    "buffer_load_dwordx4 v224, s[28:31], s16 offen lds\n"
    "ds_read_b64_tr_b16 v[76:77], v52 offset:53248\n"
    "s_waitcnt lgkmcnt(8)\n"
    "v_mfma_f32_16x16x32_bf16 v[102:105], v[126:129], a[172:175], v[102:105]\n"
    "ds_read_b64_tr_b16 v[78:79], v49 offset:53248\n"
    "ds_read_b64_tr_b16 v[80:81], v53 offset:53248\n"
    "s_waitcnt vmcnt(12)\n"
    "v_mfma_f32_16x16x32_bf16 v[90:93], v[14:17], a[48:51], v[90:93]\n"
    "ds_read_b64_tr_b16 v[82:83], v46 offset:8192\n"
    "ds_read_b64_tr_b16 v[84:85], v50 offset:8192\n"
    "v_mfma_f32_16x16x32_bf16 v[94:97], v[18:21], a[48:51], v[94:97]\n"
    "ds_read_b64_tr_b16 v[86:87], v47 offset:8192\n"
    "buffer_load_dwordx4 v225, s[28:31], s16 offen offset:1024 lds\n"
    "ds_read_b64_tr_b16 v[88:89], v51 offset:8192\n"
    "v_mfma_f32_16x16x32_bf16 v[98:101], v[38:41], a[48:51], v[98:101]\n"
    "ds_read_b64_tr_b16 v[122:123], v48 offset:8192\n"
    "ds_read_b64_tr_b16 v[124:125], v52 offset:8192\n"
    "s_waitcnt lgkmcnt(9)\n"
    "v_mfma_f32_16x16x32_bf16 v[102:105], v[54:57], a[48:51], v[102:105]\n"
    "ds_read_b64_tr_b16 v[126:127], v49 offset:8192\n"
    "ds_read_b64_tr_b16 v[128:129], v53 offset:8192\n"
    "s_waitcnt vmcnt(12)\n"
    "v_mfma_f32_16x16x32_bf16 v[90:93], v[66:69], a[112:115], v[90:93]\n"
    "ds_read_b64_tr_b16 v[14:15], v46 offset:24576\n"
    "buffer_load_dwordx4 v226, s[28:31], s16 offen offset:2048 lds\n"
    "s_add_u32 s16, s56, 0x40800\n"
    "s_add_u32 m0, s81, 0\n"
    "ds_read_b64_tr_b16 v[16:17], v50 offset:24576\n"
    "v_mfma_f32_16x16x32_bf16 v[94:97], v[70:73], a[112:115], v[94:97]\n"
    "ds_read_b64_tr_b16 v[18:19], v47 offset:24576\n"
    "ds_read_b64_tr_b16 v[20:21], v51 offset:24576\n"
    "s_waitcnt lgkmcnt(9)\n"
    "v_mfma_f32_16x16x32_bf16 v[98:101], v[74:77], a[112:115], v[98:101]\n"
    "ds_read_b64_tr_b16 v[38:39], v48 offset:24576\n"
    "ds_read_b64_tr_b16 v[40:41], v52 offset:24576\n"
    "v_mfma_f32_16x16x32_bf16 v[102:105], v[78:81], a[112:115], v[102:105]\n"
    "buffer_load_dwordx4 v224, s[28:31], s16 offen lds\n"
    "ds_read_b64_tr_b16 v[54:55], v49 offset:24576\n"
    "ds_read_b64_tr_b16 v[56:57], v53 offset:24576\n"
    "s_waitcnt vmcnt(13)\n"
    "v_mfma_f32_16x16x32_bf16 v[90:93], v[82:85], a[176:179], v[90:93]\n"
    "ds_read_b64_tr_b16 v[66:67], v46 offset:40960\n"
    "ds_read_b64_tr_b16 v[68:69], v50 offset:40960\n"
    "s_waitcnt lgkmcnt(9)\n"
    "v_mfma_f32_16x16x32_bf16 v[94:97], v[86:89], a[176:179], v[94:97]\n"
    "ds_read_b64_tr_b16 v[70:71], v47 offset:40960\n"
    "ds_read_b64_tr_b16 v[72:73], v51 offset:40960\n"
    "v_mfma_f32_16x16x32_bf16 v[98:101], v[122:125], a[176:179], v[98:101]\n"
    "buffer_load_dwordx4 v225, s[28:31], s16 offen offset:1024 lds\n"
    "ds_read_b64_tr_b16 v[74:75], v48 offset:40960\n"
    "ds_read_b64_tr_b16 v[76:77], v52 offset:40960\n"
    "v_mfma_f32_16x16x32_bf16 v[102:105], v[126:129], a[176:179], v[102:105]\n"
    "ds_read_b64_tr_b16 v[78:79], v49 offset:40960\n"
    "ds_read_b64_tr_b16 v[80:81], v53 offset:40960\n"
    "s_waitcnt vmcnt(13) lgkmcnt(10)\n"
    "v_mfma_f32_16x16x32_bf16 v[90:93], v[14:17], a[52:55], v[90:93]\n"
    "ds_read_b64_tr_b16 v[82:83], v46 offset:57344\n"
    "ds_read_b64_tr_b16 v[84:85], v50 offset:57344\n"
    "v_mfma_f32_16x16x32_bf16 v[94:97], v[18:21], a[52:55], v[94:97]\n"
    "buffer_load_dwordx4 v226, s[28:31], s16 offen offset:2048 lds\n"
    "ds_read_b64_tr_b16 v[86:87], v47 offset:57344\n"
    "ds_read_b64_tr_b16 v[88:89], v51 offset:57344\n"
    "v_mfma_f32_16x16x32_bf16 v[98:101], v[38:41], a[52:55], v[98:101]\n"
    "ds_read_b64_tr_b16 v[122:123], v48 offset:57344\n"
    "ds_read_b64_tr_b16 v[124:125], v52 offset:57344\n"
    "s_waitcnt lgkmcnt(10)\n"
    "v_mfma_f32_16x16x32_bf16 v[102:105], v[54:57], a[52:55], v[102:105]\n"
    "ds_read_b64_tr_b16 v[126:127], v49 offset:57344\n"
    "ds_read_b64_tr_b16 v[128:129], v53 offset:57344\n"
    "s_waitcnt vmcnt(13)\n"
    "v_mfma_f32_16x16x32_bf16 v[90:93], v[66:69], a[116:119], v[90:93]\n"
    "s_waitcnt vmcnt(9)\n"
    "v_cmp_ne_u64 vcc, s[88:89], v[246:247]\n"
    "s_nop 1\n"
    "s_cbranch_vccnz .Lspin_4_%=\n"
    ".Lback_5_%=:\n"
    "s_add_u32 m0, s21, 0x1e0\n"
    "ds_read_b64_tr_b16 v[14:15], v46 offset:12288\n"
    "buffer_load_dwordx4 v252, s[36:39], s14 offen sc1 lds\n"
    "ds_read_b64_tr_b16 v[16:17], v50 offset:12288\n"
    "v_mfma_f32_16x16x32_bf16 v[94:97], v[70:73], a[116:119], v[94:97]\n"
    "ds_read_b64_tr_b16 v[18:19], v47 offset:12288\n"
    "ds_read_b64_tr_b16 v[20:21], v51 offset:12288\n"
    "s_waitcnt lgkmcnt(9)\n"
    "v_mfma_f32_16x16x32_bf16 v[98:101], v[74:77], a[116:119], v[98:101]\n"
    "ds_read_b64_tr_b16 v[38:39], v48 offset:12288\n"
    "ds_read_b64_tr_b16 v[40:41], v52 offset:12288\n"
    "v_mfma_f32_16x16x32_bf16 v[102:105], v[78:81], a[116:119], v[102:105]\n"
    "ds_read_b64_tr_b16 v[54:55], v49 offset:12288\n"
    "buffer_load_dwordx4 v252, s[36:39], s14 offen offset:1024 sc1 lds\n"
    "s_add_u32 s16, s55, 0x2b000\n"
    "s_add_u32 m0, s81, 0x3000\n"
    "ds_read_b64_tr_b16 v[56:57], v53 offset:12288\n"
    "v_mfma_f32_16x16x32_bf16 v[90:93], v[82:85], a[180:183], v[90:93]\n"
    "ds_read_b64_tr_b16 v[66:67], v46 offset:28672\n"
    "ds_read_b64_tr_b16 v[68:69], v50 offset:28672\n"
    "s_waitcnt lgkmcnt(9)\n"
    "v_mfma_f32_16x16x32_bf16 v[94:97], v[86:89], a[180:183], v[94:97]\n"
    "ds_read_b64_tr_b16 v[70:71], v47 offset:28672\n"
    "ds_read_b64_tr_b16 v[72:73], v51 offset:28672\n"
    "v_mfma_f32_16x16x32_bf16 v[98:101], v[122:125], a[180:183], v[98:101]\n"
    "buffer_load_dwordx4 v224, s[28:31], s16 offen lds\n"
    "ds_read_b64_tr_b16 v[74:75], v48 offset:28672\n"
    "ds_read_b64_tr_b16 v[76:77], v52 offset:28672\n"
    "v_mfma_f32_16x16x32_bf16 v[102:105], v[126:129], a[180:183], v[102:105]\n"
    "ds_read_b64_tr_b16 v[78:79], v49 offset:28672\n"
    "ds_read_b64_tr_b16 v[80:81], v53 offset:28672\n"
    "s_waitcnt lgkmcnt(9)\n"
    "v_mfma_f32_16x16x32_bf16 v[90:93], v[14:17], a[56:59], v[90:93]\n"
    "ds_read_b64_tr_b16 v[82:83], v46 offset:45056\n"
    "ds_read_b64_tr_b16 v[84:85], v50 offset:45056\n"
    "v_mfma_f32_16x16x32_bf16 v[94:97], v[18:21], a[56:59], v[94:97]\n"
    "buffer_load_dwordx4 v225, s[28:31], s16 offen offset:1024 lds\n"
    "ds_read_b64_tr_b16 v[86:87], v47 offset:45056\n"
    "ds_read_b64_tr_b16 v[88:89], v51 offset:45056\n"
    "v_mfma_f32_16x16x32_bf16 v[98:101], v[38:41], a[56:59], v[98:101]\n"
    "ds_read_b64_tr_b16 v[122:123], v48 offset:45056\n"
    "ds_read_b64_tr_b16 v[124:125], v52 offset:45056\n"
    "s_waitcnt lgkmcnt(10)\n"
    "v_mfma_f32_16x16x32_bf16 v[102:105], v[54:57], a[56:59], v[102:105]\n"
    "ds_read_b64_tr_b16 v[126:127], v49 offset:45056\n"
    "ds_read_b64_tr_b16 v[128:129], v53 offset:45056\n"
    "v_mfma_f32_16x16x32_bf16 v[90:93], v[66:69], a[120:123], v[90:93]\n"
    "buffer_load_dwordx4 v226, s[28:31], s16 offen offset:2048 lds\n"
    "s_add_u32 s16, s55, 0x40800\n"
    "s_add_u32 m0, s81, 0x6000\n"
    "ds_read_b64_tr_b16 v[14:15], v46 offset:61440\n"
    "ds_read_b64_tr_b16 v[16:17], v50 offset:61440\n"
    "v_mfma_f32_16x16x32_bf16 v[94:97], v[70:73], a[120:123], v[94:97]\n"
    "ds_read_b64_tr_b16 v[18:19], v47 offset:61440\n"
    "ds_read_b64_tr_b16 v[20:21], v51 offset:61440\n"
    "s_waitcnt lgkmcnt(10)\n"
    "v_mfma_f32_16x16x32_bf16 v[98:101], v[74:77], a[120:123], v[98:101]\n"
    "ds_read_b64_tr_b16 v[38:39], v48 offset:61440\n"
    "ds_read_b64_tr_b16 v[40:41], v52 offset:61440\n"
    "buffer_load_dwordx4 v224, s[28:31], s16 offen lds\n"
    "v_mfma_f32_16x16x32_bf16 v[102:105], v[78:81], a[120:123], v[102:105]\n"
    "ds_read_b64_tr_b16 v[54:55], v49 offset:61440\n"
    "ds_read_b64_tr_b16 v[56:57], v53 offset:61440\n"
    "s_waitcnt vmcnt(14)\n"
    "v_mfma_f32_16x16x32_bf16 v[90:93], v[82:85], a[184:187], v[90:93]\n"
    "s_waitcnt lgkmcnt(8)\n"
    "v_mfma_f32_16x16x32_bf16 v[94:97], v[86:89], a[184:187], v[94:97]\n"
    "v_mfma_f32_16x16x32_bf16 v[98:101], v[122:125], a[184:187], v[98:101]\n"
    "buffer_load_dwordx4 v225, s[28:31], s16 offen offset:1024 lds\n"
    "v_mfma_f32_16x16x32_bf16 v[102:105], v[126:129], a[184:187], v[102:105]\n"
    "s_waitcnt vmcnt(14) lgkmcnt(4)\n"
    "v_mfma_f32_16x16x32_bf16 v[90:93], v[14:17], v[118:121], v[90:93]\n"
    "v_mfma_f32_16x16x32_bf16 v[94:97], v[18:21], v[118:121], v[94:97]\n"
    "s_waitcnt lgkmcnt(2)\n"
    "v_mfma_f32_16x16x32_bf16 v[98:101], v[38:41], v[118:121], v[98:101]\n"
    "buffer_load_dwordx4 v226, s[28:31], s16 offen offset:2048 lds\n"
    "s_add_u32 m0, s81, 0x9000\n"
    "s_waitcnt lgkmcnt(0)\n"
    "v_mfma_f32_16x16x32_bf16 v[102:105], v[54:57], v[118:121], v[102:105]\n"
    "s_lshl_b32 s20, s8, 8\n"
    "v_cvt_pk_bf16_f32 v14, v90, v91\n"
    "v_cvt_pk_bf16_f32 v15, v92, v93\n"
    "v_cvt_pk_bf16_f32 v16, v94, v95\n"
    "v_cvt_pk_bf16_f32 v17, v96, v97\n"
    "v_cvt_pk_bf16_f32 v18, v98, v99\n"
    "v_cvt_pk_bf16_f32 v19, v100, v101\n"
    "s_add_u32 s20, s20, 0x3000\n"
    "v_cvt_pk_bf16_f32 v20, v102, v103\n"
    "v_cvt_pk_bf16_f32 v21, v104, v105\n"
    "v_mfma_f32_16x16x32_bf16 v[46:49], v[14:17], v[14:17], 0\n"
    "s_lshl2_add_u32 s21, s6, s3\n"
    "s_lshl3_add_u32 s21, s21, s20\n"
    "v_mfma_f32_16x16x32_bf16 v[46:49], v[18:21], v[18:21], v[46:49]\n"
    "buffer_load_dwordx4 v224, s[28:31], s56 offen lds\n"
    "v_mfma_f32_16x16x32_bf16 v[38:41], v[22:25], v[14:17], 0\n"
    "s_add_u32 s26, s80, 0x21000\n"
    "v_add_u32 v253, s26, v252\n"
    "s_xor_b32 s26, s80, 0x1000\n"
    "s_add_u32 s26, s26, 0x21000\n"
    "v_add_u32 v250, s26, v252\n"
    "v_and_b32 v6, 1, v251\n"
    "v_lshlrev_b32 v6, 9, v6\n"
    "v_lshl_add_u32 v6, v244, 5, v6\n"
    "v_lshrrev_b32 v7, 1, v251\n"
    "v_lshl_add_u32 v245, v7, 4, v6\n"
    "s_lshl_b32 s26, s79, 12\n"
    "s_lshl_b32 s27, s10, 11\n"
    "s_add_u32 s26, s26, s27\n"
    "s_add_u32 s24, s26, 0x440000\n"
    "buffer_store_dwordx4 v[14:17], v252, s[36:39], s61 offen sc1\n"
    "v_mfma_f32_16x16x32_bf16 v[38:41], v[22:25], v[18:21], v[38:41]\n"
    "s_waitcnt vmcnt(10)\n"
    "s_barrier\n"
    "ds_read_b64_tr_b16 a[36:37], v221 offset:49152\n"
    "ds_read_b64_tr_b16 a[100:101], v220 offset:49152\n"
    "ds_read_b64_tr_b16 a[164:165], v220 offset:50176\n"
    "ds_read_b64_tr_b16 a[38:39], v221 offset:55296\n"
    "ds_read_b64_tr_b16 a[102:103], v220 offset:55296\n"
    "ds_read_b64_tr_b16 a[166:167], v220 offset:56320\n"
    "ds_read_b64_tr_b16 a[60:61], v221\n"
    "ds_read_b64_tr_b16 a[124:125], v220\n"
    "buffer_store_dwordx4 v[18:21], v252, s[36:39], s61 offen offset:1024 sc1\n"
    "v_lshrrev_b32 v2, 2, v244\n"
    "v_cmp_eq_u32 vcc, v2, v251\n"
    "v_lshlrev_b32 v4, 3, v244\n"
    "v_mov_b32 v5, 0x7f000000\n"
    "v_cndmask_b32 v4, v5, v4, vcc\n"
    "v_cndmask_b32 v2, v46, v47, s[72:73]\n"
    "v_cndmask_b32 v3, v48, v49, s[72:73]\n"
    "v_cndmask_b32 v39, v2, v3, s[74:75]\n"
    "v_mfma_f32_16x16x32_bf16 v[26:29], a[28:31], v[22:25], v[26:29]\n"
    "ds_read_b64_tr_b16 a[188:189], v220 offset:1024\n"
    "ds_read_b64_tr_b16 a[62:63], v221 offset:6144\n"
    "ds_read_b64_tr_b16 a[126:127], v220 offset:6144\n"
    "ds_read_b64_tr_b16 a[190:191], v220 offset:7168\n"
    "v_mfma_f32_16x16x32_bf16 v[34:37], a[92:95], v[22:25], v[34:37]\n"
    "buffer_store_dwordx2 v[38:39], v4, s[36:39], s62 offen sc1\n"
    "v_lshlrev_b32 v8, 5, v244\n"
    "v_lshl_add_u32 v8, v251, 3, v8\n"
    "s_add_u32 s27, s27, s93\n"
    "v_add_u32 v8, s27, v8\n"
    "s_waitcnt vmcnt(10)\n"
    "s_barrier\n"
    "ds_read2_b32 v[50:51], v58 offset0:176 offset1:177\n"
    "v_add_u32 v243, 0x10800, v252\n"
    "s_lshr_b32 s26, s80, 2\n"
    "s_add_u32 s26, s26, 0x25000\n"
    "ds_read2_b32 v[52:53], v58 offset0:178 offset1:179\n"
    "v_add_u32 v242, s26, v252\n"
    "v_mfma_f32_16x16x32_bf16 v[114:117], a[156:159], v[22:25], v[114:117]\n"
    "buffer_load_dwordx4 v225, s[28:31], s56 offen offset:1024 lds\n"
    "ds_read_b128 v[122:125], v209 offset:192\n"
    "v_and_b32 v7, 1, v244\n"
    "v_mfma_f32_16x16x32_bf16 v[26:29], a[32:35], v[22:25], v[26:29]\n"
    "ds_read_b128 v[126:129], v209 offset:704\n"
    "v_lshlrev_b32 v7, 9, v7\n"
    "s_waitcnt lgkmcnt(4)\n"
    "s_barrier\n"
    "ds_read_b128 v[130:133], v209 offset:1216\n"
    "v_and_b32 v9, 14, v244\n"
    "v_mfma_f32_16x16x32_bf16 v[34:37], a[96:99], v[22:25], v[34:37]\n"
    "ds_read_b128 v[134:137], v209 offset:1728\n"
    "v_lshl_add_u32 v7, v9, 3, v7\n"
    "v_mfma_f32_16x16x32_bf16 v[114:117], a[160:163], v[22:25], v[114:117]\n"
    "buffer_load_dwordx4 v226, s[28:31], s56 offen offset:2048 lds\n"
    "v_add_u32 v12, 0xc000, v12\n"
    "v_add_u32 v13, 0xc000, v13\n"
    "s_lshl_b32 s16, s3, 3\n"
    "s_add_u32 s16, s16, 0x80000\n"
    "s_bfe_u32 s46, 0x21170c00, s16\n"
    "s_lshl_b32 s46, s46, 15\n"
    "s_bfe_u32 s47, 0x261c1206, s16\n"
    "s_lshl_b32 s47, s47, 15\n"
    "s_bfe_u32 s48, 0x25160b04, s16\n"
    "s_lshl_b32 s48, s48, 15\n"
    "s_bfe_u32 s49, 0x2a1b1005, s16\n"
    "s_lshl_b32 s49, s49, 15\n"
    "s_bfe_u32 s50, 0x2f20110a, s16\n"
    "s_lshl_b32 s50, s50, 15\n"
    "s_bitcmp1_b32 s7, 1\n"
    "s_cselect_b32 s45, s47, s46\n"
    "s_and_b32 s16, s7, 1\n"
    "s_lshl_b32 s16, s16, 15\n"
    "s_add_u32 s45, s45, s16\n"
    "s_add_u32 s16, s7, 1\n"
    "s_bitcmp1_b32 s16, 1\n"
    "s_cselect_b32 s51, s47, s46\n"
    "s_and_b32 s16, s16, 1\n"
    "s_lshl_b32 s16, s16, 15\n"
    "s_add_u32 s51, s51, s16\n"
    "s_add_u32 s16, s7, 2\n"
    "s_bitcmp1_b32 s16, 1\n"
    "s_cselect_b32 s52, s47, s46\n"
    "s_and_b32 s16, s16, 1\n"
    "s_lshl_b32 s16, s16, 15\n"
    "s_add_u32 s52, s52, s16\n"
    "s_add_u32 s16, s7, 3\n"
    "s_bitcmp1_b32 s16, 1\n"
    "s_cselect_b32 s53, s47, s46\n"
    "s_and_b32 s16, s16, 1\n"
    "s_lshl_b32 s16, s16, 15\n"
    "s_add_u32 s53, s53, s16\n"
    "s_add_u32 s16, s56, 0x15800\n"
    "s_add_u32 m0, s81, 0xc000\n"
    "ds_read_b128 v[138:141], v209 offset:2240\n"
    "buffer_load_dwordx4 v224, s[28:31], s16 offen lds\n"
    "ds_read_b128 v[142:145], v209 offset:2752\n"
    "v_add_u32 v241, 0x25000, v7\n"
    "s_lshl1_add_u32 s26, s6, s11\n"
    "s_lshl_b32 s27, s12, 2\n"
    "ds_read_b128 v[146:149], v209 offset:3264\n"
    "s_sub_u32 s26, s26, s27\n"
    "s_and_b32 s26, s26, 15\n"
    "s_lshl_b32 s26, s26, 10\n"
    "ds_read_b128 v[150:153], v209 offset:3776\n"
    "s_lshl3_add_u32 s26, s10, s26\n"
    "v_mfma_f32_16x16x32_bf16 v[26:29], a[36:39], v[22:25], v[26:29]\n"
    "ds_read2_b32 v[54:55], v58 offset0:48 offset1:49\n"
    "v_add_u32 v239, s26, v243\n"
    "v_mfma_f32_16x16x32_bf16 v[34:37], a[100:103], v[22:25], v[34:37]\n"
    "buffer_load_dwordx4 v225, s[28:31], s16 offen offset:1024 lds\n"
    "ds_read2_b32 v[56:57], v58 offset0:50 offset1:51\n"
    "v_and_b32 v6, 1, v251\n"
    "v_mfma_f32_16x16x32_bf16 v[114:117], a[164:167], v[22:25], v[114:117]\n"
    "ds_read2_b32 v[66:67], v59 offset0:176 offset1:177\n"
    "v_lshlrev_b32 v6, 30, v6\n"
    "v_mfma_f32_16x16x32_bf16 v[26:29], a[60:63], v[22:25], v[26:29]\n"
    "ds_read2_b32 v[68:69], v59 offset0:178 offset1:179\n"
    "v_lshl_add_u32 v6, v244, 4, v6\n"
    "v_mfma_f32_16x16x32_bf16 v[34:37], a[124:127], v[22:25], v[34:37]\n"
    "ds_read2_b32 v[70:71], v59 offset0:48 offset1:49\n"
    "v_lshrrev_b32 v9, 1, v251\n"
    "v_mfma_f32_16x16x32_bf16 v[114:117], a[188:191], v[22:25], v[114:117]\n"
    "buffer_load_dwordx4 v226, s[28:31], s16 offen offset:2048 lds\n"
    "s_add_u32 s16, s56, 0x2b000\n"
    "s_add_u32 m0, s81, 0\n"
    "ds_read2_b32 v[72:73], v59 offset0:50 offset1:51\n"
    "v_lshl_add_u32 v240, v9, 8, v6\n"
    "s_mul_i32 s26, s8, 0x2b000\n"
    "s_add_u32 s57, s26, 0x840000\n"
    "ds_read2_b32 v[74:75], v60 offset0:176 offset1:177\n"
    "s_mul_i32 s26, s3, 0xc00\n"
    "s_add_u32 s26, s26, 0x8000\n"
    "s_add_u32 s86, s57, s26\n"
    "ds_read2_b32 v[76:77], v60 offset0:178 offset1:179\n"
    "s_add_u32 s90, s86, 0x800\n"
    "s_cmp_eq_u32 s3, 3\n"
    "s_cselect_b32 s90, 0x7f000000, s90\n"
    "ds_read2_b32 v[78:79], v60 offset0:48 offset1:49\n"
    "s_lshl1_add_u32 s27, s80, s57\n"
    "buffer_load_dwordx4 v224, s[28:31], s16 offen lds\n"
    "ds_read2_b32 v[80:81], v60 offset0:50 offset1:51\n"
    "s_lshl_b32 s26, s7, 11\n"
    "s_add_u32 s57, s27, s26\n"
    "s_add_u32 s26, s7, 1\n"
    "ds_read2_b32 v[82:83], v61 offset0:176 offset1:177\n"
    "s_and_b32 s26, s26, 3\n"
    "s_lshl_b32 s26, s26, 11\n"
    "s_add_u32 s84, s27, s26\n"
    "ds_read2_b32 v[84:85], v61 offset0:178 offset1:179\n"
    "s_add_u32 s26, s7, 2\n"
    "s_and_b32 s26, s26, 3\n"
    "s_lshl_b32 s26, s26, 11\n"
    "ds_read2_b32 v[86:87], v61 offset0:48 offset1:49\n"
    "s_add_u32 s91, s27, s26\n"
    "s_add_u32 s26, s7, 3\n"
    "s_and_b32 s26, s26, 3\n"
    "buffer_load_dwordx4 v225, s[28:31], s16 offen offset:1024 lds\n"
    "ds_read2_b32 v[88:89], v61 offset0:50 offset1:51\n"
    "s_waitcnt lgkmcnt(7)\n"
    "v_mfma_f32_16x16x32_bf16 v[154:157], v[50:53], v[122:125], 0\n"
    "ds_read2_b32 v[14:15], v58 offset0:160 offset1:161\n"
    "s_lshl_b32 s26, s26, 11\n"
    "v_mfma_f32_16x16x32_bf16 v[158:161], v[50:53], v[126:129], 0\n"
    "ds_read2_b32 v[16:17], v58 offset0:162 offset1:163\n"
    "s_add_u32 s94, s27, s26\n"
    "v_mfma_f32_16x16x32_bf16 v[162:165], v[50:53], v[130:133], 0\n"
    "v_and_b32 v6, 0x7f, v10\n"
    "v_lshrrev_b32 v7, 3, v255\n"
    "v_mfma_f32_16x16x32_bf16 v[166:169], v[50:53], v[134:137], 0\n"
    "buffer_load_dwordx4 v226, s[28:31], s16 offen offset:2048 lds\n"
    "s_add_u32 m0, s80, 0x10800\n"
    "ds_read_b128 v[102:105], v209 offset:128\n"
    "v_mfma_f32_16x16x32_bf16 v[170:173], v[50:53], v[138:141], 0\n"
    "ds_read_b128 v[118:121], v209 offset:640\n"
    "v_mfma_f32_16x16x32_bf16 v[174:177], v[50:53], v[142:145], 0\n"
    "v_lshl_add_u32 v216, v7, 12, v6\n"
    "v_xor_b32 v217, 64, v216\n"
    "v_mfma_f32_16x16x32_bf16 v[178:181], v[50:53], v[146:149], 0\n"
    "ds_read_b128 v[186:189], v209 offset:1152\n"
    "v_add_u32 v217, 0x7c00, v217\n"
    "v_mfma_f32_16x16x32_bf16 v[182:185], v[50:53], v[150:153], 0\n"
    "ds_read_b128 v[190:193], v209 offset:1664\n"
    "s_waitcnt vmcnt(8)\n"
    "buffer_store_dwordx2 v[222:223], v227, s[36:39], s21 offen sc1\n"
    "s_lshl2_add_u32 s18, s8, s3\n"
    "s_lshl_b32 s18, s18, 10\n"
    "s_add_u32 s18, s18, 0xdc0000\n"
    "s_add_u32 s19, s3, s12\n"
    "s_and_b32 s19, s19, 3\n"
    "s_lshl4_add_u32 s19, s8, s19\n"
    "s_lshl_b32 s19, s19, 12\n"
    "s_add_u32 s19, s19, 0x240000\n"
    "v_mfma_f32_16x16x32_bf16 v[158:161], v[54:57], v[122:125], v[158:161]\n"
    "s_lshl2_add_u32 s26, s6, s3\n"
    "s_lshl_b32 s26, s26, 16\n"
    "buffer_load_dwordx4 v10, s[32:35], s45 offen lds\n"
    "v_mfma_f32_16x16x32_bf16 v[162:165], v[54:57], v[126:129], v[162:165]\n"
    "ds_read_b128 v[194:197], v209 offset:2176\n"
    "s_lshl_b32 s27, s8, 7\n"
    "v_mfma_f32_16x16x32_bf16 v[166:169], v[54:57], v[130:133], v[166:169]\n"
    "ds_read_b128 v[198:201], v209 offset:2688\n"
    "s_add_u32 s26, s26, s27\n"
    "v_mfma_f32_16x16x32_bf16 v[170:173], v[54:57], v[134:137], v[170:173]\n"
    "s_add_u32 s58, s26, 0x440000\n"
    "s_lshr_b32 s27, s80, 1\n"
    "v_mfma_f32_16x16x32_bf16 v[174:177], v[54:57], v[138:141], v[174:177]\n"
    "ds_read_b128 v[202:205], v209 offset:3200\n"
    "s_add_u32 s59, s27, 0x26000\n"
    "buffer_load_dwordx4 v11, s[32:35], s45 offen offset:1024 lds\n"
    "v_mfma_f32_16x16x32_bf16 v[178:181], v[54:57], v[142:145], v[178:181]\n"
    "ds_read_b128 v[210:213], v209 offset:3712\n"
    "v_add_u32 v238, s59, v235\n"
    "v_mfma_f32_16x16x32_bf16 v[182:185], v[54:57], v[146:149], v[182:185]\n"
    "s_xor_b32 s26, s3, 1\n"
    "s_mul_i32 s26, s26, 0xc00\n"
    "v_mfma_f32_16x16x32_bf16 v[162:165], v[66:69], v[122:125], v[162:165]\n"
    "ds_read2_b32 v[18:19], v58 offset0:32 offset1:33\n"
    "s_add_u32 s26, s26, 0x18800\n"
    "v_mfma_f32_16x16x32_bf16 v[166:169], v[66:69], v[126:129], v[166:169]\n"
    "ds_read2_b32 v[20:21], v58 offset0:34 offset1:35\n"
    "v_add_u32 v234, s26, v252\n"
    "v_mfma_f32_16x16x32_bf16 v[170:173], v[66:69], v[130:133], v[170:173]\n"
    "buffer_load_dwordx4 v12, s[32:35], s45 offen offset:2048 lds\n"
    "v_mfma_f32_16x16x32_bf16 v[174:177], v[66:69], v[134:137], v[174:177]\n"
    "ds_read2_b32 v[30:31], v59 offset0:160 offset1:161\n"
    "s_xor_b32 s26, s3, 2\n"
    "v_mfma_f32_16x16x32_bf16 v[178:181], v[66:69], v[138:141], v[178:181]\n"
    "ds_read2_b32 v[32:33], v59 offset0:162 offset1:163\n"
    "s_mul_i32 s26, s26, 0xc00\n"
    "v_mfma_f32_16x16x32_bf16 v[182:185], v[66:69], v[142:145], v[182:185]\n"
    "s_add_u32 s26, s26, 0x18c00\n"
    "v_add_u32 v233, s26, v252\n"
    "v_mfma_f32_16x16x32_bf16 v[166:169], v[70:73], v[122:125], v[166:169]\n"
    "ds_read2_b32 v[38:39], v59 offset0:32 offset1:33\n"
    "s_xor_b32 s26, s3, 3\n"
    "buffer_load_dwordx4 v13, s[32:35], s45 offen offset:3072 lds\n"
    "s_add_u32 s16, s45, 0x4000\n"
    "s_add_u32 m0, s80, 0x14800\n"
    "v_mfma_f32_16x16x32_bf16 v[170:173], v[70:73], v[126:129], v[170:173]\n"
    "ds_read2_b32 v[40:41], v59 offset0:34 offset1:35\n"
    "s_mul_i32 s26, s26, 0xc00\n"
    "v_mfma_f32_16x16x32_bf16 v[174:177], v[70:73], v[130:133], v[174:177]\n"
    "s_add_u32 s26, s26, 0x19000\n"
    "v_add_u32 v231, s26, v252\n"
    "v_mfma_f32_16x16x32_bf16 v[178:181], v[70:73], v[134:137], v[178:181]\n"
    "ds_read2_b32 v[46:47], v60 offset0:160 offset1:161\n"
    "s_mul_i32 s26, s3, 0xc00\n"
    "v_mfma_f32_16x16x32_bf16 v[182:185], v[70:73], v[138:141], v[182:185]\n"
    "buffer_load_dwordx4 v10, s[32:35], s16 offen lds\n"
    "ds_read2_b32 v[48:49], v60 offset0:162 offset1:163\n"
    "s_waitcnt lgkmcnt(4)\n"
    "v_mfma_f32_16x16x32_bf16 v[170:173], v[74:77], v[122:125], v[170:173]\n"
    "s_add_u32 s26, s26, 0x18800\n"
    "v_add_u32 v236, s26, v252\n"
    "v_mfma_f32_16x16x32_bf16 v[174:177], v[74:77], v[126:129], v[174:177]\n"
    "ds_read2_b32 v[90:91], v60 offset0:32 offset1:33\n"
    "v_lshlrev_b32 v237, 3, v251\n"
    "v_mfma_f32_16x16x32_bf16 v[178:181], v[74:77], v[130:133], v[178:181]\n"
    "ds_read2_b32 v[92:93], v60 offset0:34 offset1:35\n"
    "v_lshl_add_u32 v237, v244, 10, v237\n"
    "v_mfma_f32_16x16x32_bf16 v[182:185], v[74:77], v[134:137], v[182:185]\n"
    "buffer_load_dwordx4 v11, s[32:35], s16 offen offset:1024 lds\n"
    "v_mfma_f32_16x16x32_bf16 v[174:177], v[78:81], v[122:125], v[174:177]\n"
    "ds_read2_b32 v[94:95], v61 offset0:160 offset1:161\n"
    "s_lshl2_add_u32 s25, s6, s3\n"
    "v_mfma_f32_16x16x32_bf16 v[178:181], v[78:81], v[126:129], v[178:181]\n"
    "ds_read2_b32 v[96:97], v61 offset0:162 offset1:163\n"
    "s_lshl_b32 s25, s25, 5\n"
    "v_mfma_f32_16x16x32_bf16 v[182:185], v[78:81], v[130:133], v[182:185]\n"
    "s_lshl_b32 s26, s8, 16\n"
    "s_add_u32 s25, s25, s26\n"
    "v_mfma_f32_16x16x32_bf16 v[178:181], v[82:85], v[122:125], v[178:181]\n"
    "ds_read2_b32 v[98:99], v61 offset0:32 offset1:33\n"
    "buffer_load_dwordx4 v12, s[32:35], s16 offen offset:2048 lds\n"
    "v_mfma_f32_16x16x32_bf16 v[182:185], v[82:85], v[126:129], v[182:185]\n"
    "ds_read2_b32 v[100:101], v61 offset0:34 offset1:35\n"
    "v_mfma_f32_16x16x32_bf16 v[182:185], v[86:89], v[122:125], v[182:185]\n"
    "v_mfma_f32_16x16x32_bf16 v[154:157], v[14:17], v[102:105], v[154:157]\n"
    "ds_read2_b32 v[50:51], v58 offset0:144 offset1:145\n"
    "v_mfma_f32_16x16x32_bf16 v[158:161], v[14:17], v[118:121], v[158:161]\n"
    "ds_read2_b32 v[52:53], v58 offset0:146 offset1:147\n"
    "buffer_load_dwordx4 v13, s[32:35], s16 offen offset:3072 lds\n"
    "s_add_u32 m0, s80, 0x18800\n"
    "v_mfma_f32_16x16x32_bf16 v[162:165], v[14:17], v[186:189], v[162:165]\n"
    "v_mfma_f32_16x16x32_bf16 v[166:169], v[14:17], v[190:193], v[166:169]\n"
    "ds_read_b128 v[122:125], v209 offset:64\n"
    "v_mfma_f32_16x16x32_bf16 v[170:173], v[14:17], v[194:197], v[170:173]\n"
    "ds_read_b128 v[126:129], v209 offset:576\n"
    "v_mfma_f32_16x16x32_bf16 v[174:177], v[14:17], v[198:201], v[174:177]\n"
    "buffer_load_dwordx4 v10, s[32:35], s51 offen lds\n"
    "v_mfma_f32_16x16x32_bf16 v[178:181], v[14:17], v[202:205], v[178:181]\n"
    "ds_read_b128 v[130:133], v209 offset:1088\n"
    "v_mfma_f32_16x16x32_bf16 v[182:185], v[14:17], v[210:213], v[182:185]\n"
    "ds_read_b128 v[134:137], v209 offset:1600\n"
    "v_mfma_f32_16x16x32_bf16 v[158:161], v[18:21], v[102:105], v[158:161]\n"
    "v_mfma_f32_16x16x32_bf16 v[162:165], v[18:21], v[118:121], v[162:165]\n"
    "ds_read_b128 v[138:141], v209 offset:2112\n"
    "buffer_load_dwordx4 v11, s[32:35], s51 offen offset:1024 lds\n"
    "v_mfma_f32_16x16x32_bf16 v[166:169], v[18:21], v[186:189], v[166:169]\n"
    "ds_read_b128 v[142:145], v209 offset:2624\n"
    "v_mfma_f32_16x16x32_bf16 v[170:173], v[18:21], v[190:193], v[170:173]\n"
    "v_mfma_f32_16x16x32_bf16 v[174:177], v[18:21], v[194:197], v[174:177]\n"
    "ds_read_b128 v[146:149], v209 offset:3136\n"
    "v_mfma_f32_16x16x32_bf16 v[178:181], v[18:21], v[198:201], v[178:181]\n"
    "ds_read_b128 v[150:153], v209 offset:3648\n"
    "buffer_load_dwordx4 v12, s[32:35], s51 offen offset:2048 lds\n"
    "v_mfma_f32_16x16x32_bf16 v[182:185], v[18:21], v[202:205], v[182:185]\n"
    "v_mfma_f32_16x16x32_bf16 v[162:165], v[30:33], v[102:105], v[162:165]\n"
    "ds_read2_b32 v[54:55], v58 offset0:16 offset1:17\n"
    "v_mfma_f32_16x16x32_bf16 v[166:169], v[30:33], v[118:121], v[166:169]\n"
    "ds_read2_b32 v[56:57], v58 offset0:18 offset1:19\n"
    "v_mfma_f32_16x16x32_bf16 v[170:173], v[30:33], v[186:189], v[170:173]\n"
    "buffer_load_dwordx4 v13, s[32:35], s51 offen offset:3072 lds\n"
    "s_add_u32 s16, s51, 0x4000\n"
    "s_add_u32 m0, s80, 0x1c800\n"
    "v_mfma_f32_16x16x32_bf16 v[174:177], v[30:33], v[190:193], v[174:177]\n"
    "ds_read2_b32 v[66:67], v59 offset0:144 offset1:145\n"
    "v_mfma_f32_16x16x32_bf16 v[178:181], v[30:33], v[194:197], v[178:181]\n"
    "ds_read2_b32 v[68:69], v59 offset0:146 offset1:147\n"
    "v_mfma_f32_16x16x32_bf16 v[182:185], v[30:33], v[198:201], v[182:185]\n"
    "s_waitcnt lgkmcnt(4)\n"
    "v_mfma_f32_16x16x32_bf16 v[166:169], v[38:41], v[102:105], v[166:169]\n"
    "s_waitcnt vmcnt(13)\n"
    "s_barrier\n"
    "ds_read_b64_tr_b16 a[40:41], v221 offset:12288\n"
    "ds_read_b64_tr_b16 a[104:105], v220 offset:12288\n"
    "ds_read_b64_tr_b16 a[168:169], v220 offset:13312\n"
    "ds_read_b64_tr_b16 a[42:43], v221 offset:18432\n"
    "ds_read_b64_tr_b16 a[106:107], v220 offset:18432\n"
    "ds_read_b64_tr_b16 a[170:171], v220 offset:19456\n"
    "ds_read_b64_tr_b16 a[44:45], v221 offset:24576\n"
    "ds_read_b64_tr_b16 a[108:109], v220 offset:24576\n"
    "ds_read_b64_tr_b16 a[172:173], v220 offset:25600\n"
    "ds_read_b64_tr_b16 a[46:47], v221 offset:30720\n"
    "ds_read_b64_tr_b16 a[110:111], v220 offset:30720\n"
    "ds_read_b64_tr_b16 a[174:175], v220 offset:31744\n"
    "s_waitcnt vmcnt(8)\n"
    "ds_read_b64_tr_b16 a[48:49], v221 offset:36864\n"
    "ds_read_b64_tr_b16 a[112:113], v220 offset:36864\n"
    "ds_read_b64_tr_b16 a[176:177], v220 offset:37888\n"
    "ds_read_b64_tr_b16 a[50:51], v221 offset:43008\n"
    "ds_read_b64_tr_b16 a[114:115], v220 offset:43008\n"
    "ds_read_b64_tr_b16 a[178:179], v220 offset:44032\n"
    "ds_read_b64_tr_b16 a[52:53], v221 offset:49152\n"
    "ds_read_b64_tr_b16 a[116:117], v220 offset:49152\n"
    "ds_read_b64_tr_b16 a[180:181], v220 offset:50176\n"
    "ds_read_b64_tr_b16 a[54:55], v221 offset:55296\n"
    "ds_read_b64_tr_b16 a[118:119], v220 offset:55296\n"
    "ds_read_b64_tr_b16 a[182:183], v220 offset:56320\n"
    "ds_read_b64_tr_b16 a[56:57], v221\n"
    "ds_read_b64_tr_b16 a[120:121], v220\n"
    "ds_read_b64_tr_b16 a[184:185], v220 offset:1024\n"
    "ds_read_b64_tr_b16 a[58:59], v221 offset:6144\n"
    "ds_read_b64_tr_b16 a[122:123], v220 offset:6144\n"
    "ds_read_b64_tr_b16 a[186:187], v220 offset:7168\n"
    "ds_read_b64_tr_b16 a[192:193], v62\n"
    "ds_read_b64_tr_b16 a[194:195], v62 offset:2048\n"
    "ds_read_b64_tr_b16 a[196:197], v63\n"
    "ds_read_b64_tr_b16 a[198:199], v63 offset:2048\n"
    "ds_read_b64_tr_b16 a[200:201], v64\n"
    "ds_read_b64_tr_b16 a[202:203], v64 offset:2048\n"
    "ds_read_b64_tr_b16 a[204:205], v65\n"
    "ds_read_b64_tr_b16 a[206:207], v65 offset:2048\n"
    "buffer_load_dwordx4 v10, s[32:35], s16 offen lds\n"
    "ds_read2_b32 v[70:71], v59 offset0:16 offset1:17\n"
    "v_mfma_f32_16x16x32_bf16 v[170:173], v[38:41], v[118:121], v[170:173]\n"
    "ds_read2_b32 v[72:73], v59 offset0:18 offset1:19\n"
    "v_mfma_f32_16x16x32_bf16 v[174:177], v[38:41], v[186:189], v[174:177]\n"
    "v_mfma_f32_16x16x32_bf16 v[178:181], v[38:41], v[190:193], v[178:181]\n"
    "s_waitcnt lgkmcnt(10)\n"
    "s_barrier\n"
    "ds_read2_b32 v[74:75], v60 offset0:144 offset1:145\n"
    "v_mfma_f32_16x16x32_bf16 v[182:185], v[38:41], v[194:197], v[182:185]\n"
    "buffer_load_dwordx4 v11, s[32:35], s16 offen offset:1024 lds\n"
    "ds_read2_b32 v[76:77], v60 offset0:146 offset1:147\n"
    "v_mfma_f32_16x16x32_bf16 v[170:173], v[46:49], v[102:105], v[170:173]\n"
    "s_waitcnt vmcnt(6)\n"
    "ds_read_b64_tr_b16 a[208:209], v62 offset:16384\n"
    "ds_read_b64_tr_b16 a[210:211], v62 offset:18432\n"
    "ds_read_b64_tr_b16 a[212:213], v63 offset:16384\n"
    "ds_read_b64_tr_b16 a[214:215], v63 offset:18432\n"
    "ds_read_b64_tr_b16 a[216:217], v64 offset:16384\n"
    "ds_read_b64_tr_b16 a[218:219], v64 offset:18432\n"
    "ds_read_b64_tr_b16 a[220:221], v65 offset:16384\n"
    "ds_read_b64_tr_b16 a[222:223], v65 offset:18432\n"
    "s_waitcnt lgkmcnt(10)\n"
    "buffer_load_dwordx4 v12, s[32:35], s16 offen offset:2048 lds\n"
    "v_mfma_f32_16x16x32_bf16 v[174:177], v[46:49], v[118:121], v[174:177]\n"
    "ds_read2_b32 v[78:79], v60 offset0:16 offset1:17\n"
    "v_mfma_f32_16x16x32_bf16 v[178:181], v[46:49], v[186:189], v[178:181]\n"
    "ds_read2_b32 v[80:81], v60 offset0:18 offset1:19\n"
    "v_mfma_f32_16x16x32_bf16 v[182:185], v[46:49], v[190:193], v[182:185]\n"
    "v_mfma_f32_16x16x32_bf16 v[174:177], v[90:93], v[102:105], v[174:177]\n"
    "ds_read2_b32 v[82:83], v61 offset0:144 offset1:145\n"
    "buffer_load_dwordx4 v13, s[32:35], s16 offen offset:3072 lds\n"
    "v_mfma_f32_16x16x32_bf16 v[178:181], v[90:93], v[118:121], v[178:181]\n"
    "ds_read2_b32 v[84:85], v61 offset0:146 offset1:147\n"
    "v_mfma_f32_16x16x32_bf16 v[182:185], v[90:93], v[186:189], v[182:185]\n"
    "v_mfma_f32_16x16x32_bf16 v[178:181], v[94:97], v[102:105], v[178:181]\n"
    "ds_read2_b32 v[86:87], v61 offset0:16 offset1:17\n"
    "v_mfma_f32_16x16x32_bf16 v[182:185], v[94:97], v[118:121], v[182:185]\n"
    "ds_read2_b32 v[88:89], v61 offset0:18 offset1:19\n"
    "buffer_load_dwordx2 v[248:249], v228, s[36:39], s20 offen sc1\n"
    "s_add_u32 m0, s80, 0\n"
    "v_mfma_f32_16x16x32_bf16 v[182:185], v[98:101], v[102:105], v[182:185]\n"
    "v_mfma_f32_16x16x32_bf16 v[154:157], v[50:53], v[122:125], v[154:157]\n"
    "ds_read2_b32 v[14:15], v58 offset0:128 offset1:129\n"
    "v_mfma_f32_16x16x32_bf16 v[158:161], v[50:53], v[126:129], v[158:161]\n"
    "ds_read2_b32 v[16:17], v58 offset0:130 offset1:131\n"
    "buffer_load_dwordx4 v10, s[32:35], s52 offen lds\n"
    "v_mfma_f32_16x16x32_bf16 v[162:165], v[50:53], v[130:133], v[162:165]\n"
    "s_waitcnt vmcnt(6)\n"
    "ds_read_b64_tr_b16 a[224:225], v62 offset:32768\n"
    "ds_read_b64_tr_b16 a[226:227], v62 offset:34816\n"
    "ds_read_b64_tr_b16 a[228:229], v63 offset:32768\n"
    "ds_read_b64_tr_b16 a[230:231], v63 offset:34816\n"
    "ds_read_b64_tr_b16 a[232:233], v64 offset:32768\n"
    "ds_read_b64_tr_b16 a[234:235], v64 offset:34816\n"
    "ds_read_b64_tr_b16 a[236:237], v65 offset:32768\n"
    "ds_read_b64_tr_b16 a[238:239], v65 offset:34816\n"
    "s_waitcnt lgkmcnt(10)\n"
    "buffer_load_dwordx4 v11, s[32:35], s52 offen offset:1024 lds\n"
    "v_mfma_f32_16x16x32_bf16 v[166:169], v[50:53], v[134:137], v[166:169]\n"
    "ds_read_b128 v[102:105], v209\n"
    "v_mfma_f32_16x16x32_bf16 v[170:173], v[50:53], v[138:141], v[170:173]\n"
    "ds_read_b128 v[118:121], v209 offset:512\n"
    "v_mfma_f32_16x16x32_bf16 v[174:177], v[50:53], v[142:145], v[174:177]\n"
    "v_mfma_f32_16x16x32_bf16 v[178:181], v[50:53], v[146:149], v[178:181]\n"
    "ds_read_b128 v[186:189], v209 offset:1024\n"
    "buffer_load_dwordx4 v12, s[32:35], s52 offen offset:2048 lds\n"
    "v_mfma_f32_16x16x32_bf16 v[182:185], v[50:53], v[150:153], v[182:185]\n"
    "ds_read_b128 v[190:193], v209 offset:1536\n"
    "v_mfma_f32_16x16x32_bf16 v[158:161], v[54:57], v[122:125], v[158:161]\n"
    "v_mfma_f32_16x16x32_bf16 v[162:165], v[54:57], v[126:129], v[162:165]\n"
    "ds_read_b128 v[194:197], v209 offset:2048\n"
    "v_mfma_f32_16x16x32_bf16 v[166:169], v[54:57], v[130:133], v[166:169]\n"
    "ds_read_b128 v[198:201], v209 offset:2560\n"
    "buffer_load_dwordx4 v13, s[32:35], s52 offen offset:3072 lds\n"
    "s_add_u32 s16, s52, 0x4000\n"
    "s_add_u32 m0, s80, 0x4000\n"
    "v_mfma_f32_16x16x32_bf16 v[170:173], v[54:57], v[134:137], v[170:173]\n"
    "v_mfma_f32_16x16x32_bf16 v[174:177], v[54:57], v[138:141], v[174:177]\n"
    "ds_read_b128 v[202:205], v209 offset:3072\n"
    "v_mfma_f32_16x16x32_bf16 v[178:181], v[54:57], v[142:145], v[178:181]\n"
    "ds_read_b128 v[210:213], v209 offset:3584\n"
    "v_mfma_f32_16x16x32_bf16 v[182:185], v[54:57], v[146:149], v[182:185]\n"
    "buffer_load_dwordx4 v10, s[32:35], s16 offen lds\n"
    "v_mfma_f32_16x16x32_bf16 v[162:165], v[66:69], v[122:125], v[162:165]\n"
    "ds_read2_b32 v[18:19], v58 offset0:0 offset1:1\n"
    "v_mfma_f32_16x16x32_bf16 v[166:169], v[66:69], v[126:129], v[166:169]\n"
    "ds_read2_b32 v[20:21], v58 offset0:2 offset1:3\n"
    "v_mfma_f32_16x16x32_bf16 v[170:173], v[66:69], v[130:133], v[170:173]\n"
    "v_mfma_f32_16x16x32_bf16 v[174:177], v[66:69], v[134:137], v[174:177]\n"
    "ds_read2_b32 v[30:31], v59 offset0:128 offset1:129\n"
    "buffer_load_dwordx4 v11, s[32:35], s16 offen offset:1024 lds\n"
    "v_mfma_f32_16x16x32_bf16 v[178:181], v[66:69], v[138:141], v[178:181]\n"
    "ds_read2_b32 v[32:33], v59 offset0:130 offset1:131\n"
    "v_mfma_f32_16x16x32_bf16 v[182:185], v[66:69], v[142:145], v[182:185]\n"
    "v_mfma_f32_16x16x32_bf16 v[166:169], v[70:73], v[122:125], v[166:169]\n"
    "ds_read2_b32 v[38:39], v59 offset0:0 offset1:1\n"
    "v_mfma_f32_16x16x32_bf16 v[170:173], v[70:73], v[126:129], v[170:173]\n"
    "ds_read2_b32 v[40:41], v59 offset0:2 offset1:3\n"
    "buffer_load_dwordx4 v12, s[32:35], s16 offen offset:2048 lds\n"
    "v_mfma_f32_16x16x32_bf16 v[174:177], v[70:73], v[130:133], v[174:177]\n"
    "v_mfma_f32_16x16x32_bf16 v[178:181], v[70:73], v[134:137], v[178:181]\n"
    "ds_read2_b32 v[46:47], v60 offset0:128 offset1:129\n"
    "v_mfma_f32_16x16x32_bf16 v[182:185], v[70:73], v[138:141], v[182:185]\n"
    "ds_read2_b32 v[48:49], v60 offset0:130 offset1:131\n"
    "v_mfma_f32_16x16x32_bf16 v[170:173], v[74:77], v[122:125], v[170:173]\n"
    "buffer_load_dwordx4 v13, s[32:35], s16 offen offset:3072 lds\n"
    "v_mfma_f32_16x16x32_bf16 v[174:177], v[74:77], v[126:129], v[174:177]\n"
    "ds_read2_b32 v[90:91], v60 offset0:0 offset1:1\n"
    "v_mfma_f32_16x16x32_bf16 v[178:181], v[74:77], v[130:133], v[178:181]\n"
    "ds_read2_b32 v[92:93], v60 offset0:2 offset1:3\n"
    "s_waitcnt vmcnt(9)\n"
    "ds_read_b64_tr_b16 a[240:241], v62 offset:49152\n"
    "ds_read_b64_tr_b16 a[242:243], v62 offset:51200\n"
    "ds_read_b64_tr_b16 a[244:245], v63 offset:49152\n"
    "ds_read_b64_tr_b16 a[246:247], v63 offset:51200\n"
    "ds_read_b64_tr_b16 a[248:249], v64 offset:49152\n"
    "ds_read_b64_tr_b16 a[250:251], v64 offset:51200\n"
    "ds_read_b64_tr_b16 a[252:253], v65 offset:49152\n"
    "ds_read_b64_tr_b16 a[254:255], v65 offset:51200\n"
    "s_waitcnt lgkmcnt(10)\n"
    "s_waitcnt vmcnt(8)\n"
    "v_cmp_ne_u64 vcc, s[88:89], v[248:249]\n"
    "v_mfma_f32_16x16x32_bf16 v[182:185], v[74:77], v[134:137], v[182:185]\n"
    "s_nop 0\n"
    "s_cbranch_vccnz .Lspin_7_%=\n"
    ".Lback_8_%=:\n"
    "s_add_u32 m0, s80, 0x10800\n"
    "s_nop 0\n"
    "buffer_load_dwordx4 v252, s[36:39], s19 offen sc1 lds\n"
    "v_mfma_f32_16x16x32_bf16 v[174:177], v[78:81], v[122:125], v[174:177]\n"
    "ds_read2_b32 v[94:95], v61 offset0:128 offset1:129\n"
    "v_mfma_f32_16x16x32_bf16 v[178:181], v[78:81], v[126:129], v[178:181]\n"
    "ds_read2_b32 v[96:97], v61 offset0:130 offset1:131\n"
    "v_mfma_f32_16x16x32_bf16 v[182:185], v[78:81], v[130:133], v[182:185]\n"
    "v_mfma_f32_16x16x32_bf16 v[178:181], v[82:85], v[122:125], v[178:181]\n"
    "ds_read2_b32 v[98:99], v61 offset0:0 offset1:1\n"
    "buffer_load_dwordx4 v252, s[36:39], s19 offen offset:1024 sc1 lds\n"
    "s_waitcnt lgkmcnt(3)\n"
    "v_mfma_f32_16x16x32_bf16 v[182:185], v[82:85], v[126:129], v[182:185]\n"
    "ds_read2_b32 v[100:101], v61 offset0:2 offset1:3\n"
    "v_mfma_f32_16x16x32_bf16 v[182:185], v[86:89], v[122:125], v[182:185]\n"
    "v_mfma_f32_16x16x32_bf16 v[154:157], v[14:17], v[102:105], v[154:157]\n"
    "v_mfma_f32_16x16x32_bf16 v[158:161], v[14:17], v[118:121], v[158:161]\n"
    "buffer_load_dwordx4 v252, s[36:39], s19 offen offset:2048 sc1 lds\n"
    "v_mfma_f32_16x16x32_bf16 v[162:165], v[14:17], v[186:189], v[162:165]\n"
    "v_mfma_f32_16x16x32_bf16 v[166:169], v[14:17], v[190:193], v[166:169]\n"
    "v_mfma_f32_16x16x32_bf16 v[170:173], v[14:17], v[194:197], v[170:173]\n"
    "v_mfma_f32_16x16x32_bf16 v[174:177], v[14:17], v[198:201], v[174:177]\n"
    "buffer_load_dwordx4 v252, s[36:39], s19 offen offset:3072 sc1 lds\n"
    "v_mfma_f32_16x16x32_bf16 v[178:181], v[14:17], v[202:205], v[178:181]\n"
    "s_waitcnt lgkmcnt(0)\n"
    "s_barrier\n"
    "v_mfma_f32_16x16x32_bf16 v[182:185], v[14:17], v[210:213], v[182:185]\n"
    "ds_read_b64 v[50:51], v8 offset:480\n"
    "v_mfma_f32_16x16x32_bf16 v[158:161], v[18:21], v[102:105], v[158:161]\n"
    "ds_read_b64 v[52:53], v8 offset:992\n"
    "v_mfma_f32_16x16x32_bf16 v[162:165], v[18:21], v[118:121], v[162:165]\n"
    "ds_read_b64 v[54:55], v8 offset:1504\n"
    "buffer_load_dwordx4 v[2:5], v252, s[36:39], s18 offen sc1\n"
    "s_add_u32 s19, s19, 0x4000\n"
    "s_add_u32 m0, s80, 0x14800\n"
    "v_mfma_f32_16x16x32_bf16 v[166:169], v[18:21], v[186:189], v[166:169]\n"
    "ds_read_b64 v[56:57], v8 offset:2016\n"
    "v_mfma_f32_16x16x32_bf16 v[170:173], v[18:21], v[190:193], v[170:173]\n"
    "v_mfma_f32_16x16x32_bf16 v[174:177], v[18:21], v[194:197], v[174:177]\n"
    "v_mfma_f32_16x16x32_bf16 v[178:181], v[18:21], v[198:201], v[178:181]\n"
    "buffer_load_dwordx4 v252, s[36:39], s19 offen sc1 lds\n"
    "v_mfma_f32_16x16x32_bf16 v[182:185], v[18:21], v[202:205], v[182:185]\n"
    "s_waitcnt lgkmcnt(3)\n"
    "v_lshlrev_b32 v66, 16, v50\n"
    "v_mfma_f32_16x16x32_bf16 v[162:165], v[30:33], v[102:105], v[162:165]\n"
    "v_and_b32 v67, s78, v50\n"
    "v_lshlrev_b32 v68, 16, v51\n"
    "v_mfma_f32_16x16x32_bf16 v[166:169], v[30:33], v[118:121], v[166:169]\n"
    "v_and_b32 v69, s78, v51\n"
    "v_mfma_f32_16x16x32_bf16 v[170:173], v[30:33], v[186:189], v[170:173]\n"
    "s_waitcnt lgkmcnt(1)\n"
    "v_lshlrev_b32 v70, 16, v52\n"
    "v_mfma_f32_16x16x32_bf16 v[174:177], v[30:33], v[190:193], v[174:177]\n"
    "buffer_load_dwordx4 v252, s[36:39], s19 offen offset:1024 sc1 lds\n"
    "v_mfma_f32_16x16x32_bf16 v[178:181], v[30:33], v[194:197], v[178:181]\n"
    "v_and_b32 v71, s78, v52\n"
    "v_lshlrev_b32 v72, 16, v53\n"
    "v_mfma_f32_16x16x32_bf16 v[182:185], v[30:33], v[198:201], v[182:185]\n"
    "v_and_b32 v73, s78, v53\n"
    "v_lshlrev_b32 v74, 16, v54\n"
    "v_mfma_f32_16x16x32_bf16 v[166:169], v[38:41], v[102:105], v[166:169]\n"
    "v_and_b32 v75, s78, v54\n"
    "v_lshlrev_b32 v76, 16, v55\n"
    "v_mfma_f32_16x16x32_bf16 v[170:173], v[38:41], v[118:121], v[170:173]\n"
    "v_and_b32 v77, s78, v55\n"
    "s_waitcnt lgkmcnt(0)\n"
    "v_lshlrev_b32 v78, 16, v56\n"
    "v_mfma_f32_16x16x32_bf16 v[174:177], v[38:41], v[186:189], v[174:177]\n"
    "buffer_load_dwordx4 v252, s[36:39], s19 offen offset:2048 sc1 lds\n"
    "v_mfma_f32_16x16x32_bf16 v[178:181], v[38:41], v[190:193], v[178:181]\n"
    "v_and_b32 v79, s78, v56\n"
    "v_lshlrev_b32 v80, 16, v57\n"
    "v_mfma_f32_16x16x32_bf16 v[182:185], v[38:41], v[194:197], v[182:185]\n"
    "v_and_b32 v81, s78, v57\n"
    "v_mfma_f32_16x16x32_bf16 v[170:173], v[46:49], v[102:105], v[170:173]\n"
    "v_mfma_f32_16x16x32_bf16 v[174:177], v[46:49], v[118:121], v[174:177]\n"
    "buffer_load_dwordx4 v252, s[36:39], s19 offen offset:3072 sc1 lds\n"
    "s_add_u32 s19, s19, 0x4000\n"
    "s_add_u32 m0, s80, 0x18800\n"
    "v_mfma_f32_16x16x32_bf16 v[178:181], v[46:49], v[186:189], v[178:181]\n"
    "v_mfma_f32_16x16x32_bf16 v[182:185], v[46:49], v[190:193], v[182:185]\n"
    "v_mfma_f32_16x16x32_bf16 v[174:177], v[90:93], v[102:105], v[174:177]\n"
    "v_mfma_f32_16x16x32_bf16 v[178:181], v[90:93], v[118:121], v[178:181]\n"
    "buffer_load_dwordx4 v252, s[36:39], s19 offen sc1 lds\n"
    "v_mfma_f32_16x16x32_bf16 v[182:185], v[90:93], v[186:189], v[182:185]\n"
    "v_mfma_f32_16x16x32_bf16 v[178:181], v[94:97], v[102:105], v[178:181]\n"
    "v_mfma_f32_16x16x32_bf16 v[182:185], v[94:97], v[118:121], v[182:185]\n"
    "v_mfma_f32_16x16x32_bf16 v[182:185], v[98:101], v[102:105], v[182:185]\n"
    "v_subrev_u32 v62, 0x10800, v62\n"
    "v_subrev_u32 v63, 0x10800, v63\n"
    "v_subrev_u32 v64, 0x10800, v64\n"
    "v_subrev_u32 v65, 0x10800, v65\n"
    "s_waitcnt vmcnt(14)\n"
    "buffer_load_dwordx4 v252, s[36:39], s19 offen offset:1024 sc1 lds\n"
    "ds_read_b64_tr_b16 v[194:195], v62\n"
    "ds_read_b64_tr_b16 v[196:197], v62 offset:2048\n"
    "ds_read_b64_tr_b16 v[198:199], v63\n"
    "ds_read_b64_tr_b16 v[200:201], v63 offset:2048\n"
    "ds_read_b64_tr_b16 v[202:203], v64\n"
    "ds_read_b64_tr_b16 v[204:205], v64 offset:2048\n"
    "ds_read_b64_tr_b16 v[206:207], v65\n"
    "ds_read_b64_tr_b16 v[208:209], v65 offset:2048\n"
    "s_waitcnt vmcnt(11)\n"
    "buffer_load_dwordx4 v252, s[36:39], s19 offen offset:2048 sc1 lds\n"
    "v_mfma_f32_16x16x32_bf16 v[26:29], a[40:43], v[22:25], v[26:29]\n"
    "s_mov_b32 s68, 0xbfb8aa3b\n"
    "v_mfma_f32_16x16x32_bf16 v[34:37], a[104:107], v[22:25], v[34:37]\n"
    "v_mfma_f32_16x16x32_bf16 v[114:117], a[168:171], v[22:25], v[114:117]\n"
    "v_mfma_f32_16x16x32_bf16 v[26:29], a[44:47], v[22:25], v[26:29]\n"
    "buffer_load_dwordx4 v252, s[36:39], s19 offen offset:3072 sc1 lds\n"
    "s_add_u32 s19, s19, 0x4000\n"
    "s_add_u32 m0, s80, 0x1c800\n"
    "s_waitcnt vmcnt(9)\n"
    "s_barrier\n"
    "ds_read_b128 v[54:57], v243\n"
    "v_mfma_f32_16x16x32_bf16 v[34:37], a[108:111], v[22:25], v[34:37]\n"
    "ds_read_b128 v[58:61], v243 offset:1024\n"
    "v_mfma_f32_16x16x32_bf16 v[114:117], a[172:175], v[22:25], v[114:117]\n"
    "ds_read_b128 v[82:85], v243 offset:2048\n"
    "v_mfma_f32_16x16x32_bf16 v[26:29], a[48:51], v[22:25], v[26:29]\n"
    "buffer_load_dwordx4 v252, s[36:39], s19 offen sc1 lds\n"
    "ds_read_b128 v[86:89], v243 offset:3072\n"
    "v_mfma_f32_16x16x32_bf16 v[34:37], a[112:115], v[22:25], v[34:37]\n"
    "ds_read_b128 v[90:93], v243 offset:4096\n"
    "buffer_load_dwordx4 v252, s[36:39], s19 offen offset:1024 sc1 lds\n"
    "s_cmp_eq_u32 s10, 0\n"
    "s_cbranch_scc1 .Lnosw_13_%=\n"
    "v_swap_b32 v154, v170\n"
    "v_swap_b32 v155, v171\n"
    "v_swap_b32 v156, v172\n"
    "v_swap_b32 v157, v173\n"
    "v_swap_b32 v158, v174\n"
    "v_swap_b32 v159, v175\n"
    "v_swap_b32 v160, v176\n"
    "v_swap_b32 v161, v177\n"
    "v_swap_b32 v162, v178\n"
    "v_swap_b32 v163, v179\n"
    "v_swap_b32 v164, v180\n"
    "v_swap_b32 v165, v181\n"
    "v_swap_b32 v166, v182\n"
    "v_swap_b32 v167, v183\n"
    "v_swap_b32 v168, v184\n"
    "v_swap_b32 v169, v185\n"
    ".Lnosw_13_%=:\n"
    "v_pk_fma_f32 v[154:155], v[66:67], v[44:45], v[154:155] op_sel:[0,1,0] op_sel_hi:[1,1,1]\n"
    "v_pk_fma_f32 v[156:157], v[68:69], v[44:45], v[156:157] op_sel:[0,1,0] op_sel_hi:[1,1,1]\n"
    "buffer_load_dwordx4 v252, s[36:39], s19 offen offset:2048 sc1 lds\n"
    "v_pk_fma_f32 v[158:159], v[70:71], v[44:45], v[158:159] op_sel:[0,1,0] op_sel_hi:[1,1,1]\n"
    "v_pk_fma_f32 v[160:161], v[72:73], v[44:45], v[160:161] op_sel:[0,1,0] op_sel_hi:[1,1,1]\n"
    "v_pk_fma_f32 v[162:163], v[74:75], v[44:45], v[162:163] op_sel:[0,1,0] op_sel_hi:[1,1,1]\n"
    "v_pk_fma_f32 v[164:165], v[76:77], v[44:45], v[164:165] op_sel:[0,1,0] op_sel_hi:[1,1,1]\n"
    "v_pk_fma_f32 v[166:167], v[78:79], v[44:45], v[166:167] op_sel:[0,1,0] op_sel_hi:[1,1,1]\n"
    "v_pk_fma_f32 v[168:169], v[80:81], v[44:45], v[168:169] op_sel:[0,1,0] op_sel_hi:[1,1,1]\n"
    "s_waitcnt lgkmcnt(0)\n"
    "v_mfma_f32_16x16x32_bf16 v[14:17], a[0:3], v[54:57], 0\n"
    "ds_read_b128 v[94:97], v243 offset:5120\n"
    "v_mfma_f32_16x16x32_bf16 v[18:21], a[64:67], v[54:57], 0\n"
    "ds_write_b128 v253, v[170:173]\n"
    "buffer_load_dwordx4 v252, s[36:39], s19 offen offset:3072 sc1 lds\n"
    "s_lshl1_add_u32 s21, s6, s10\n"
    "s_lshl_b32 s21, s21, 6\n"
    "s_lshl1_add_u32 s20, s8, s11\n"
    "s_add_u32 s21, s21, s20\n"
    "s_lshl3_add_u32 s21, s21, 0x7000\n"
    "s_add_u32 m0, s80, 0x8000\n"
    "v_mfma_f32_16x16x32_bf16 v[30:33], a[128:131], v[54:57], 0\n"
    "s_waitcnt vmcnt(12)\n"
    "v_permlane32_swap_b32 v2, v4\n"
    "v_mfma_f32_16x16x32_bf16 v[14:17], a[4:7], v[58:61], v[14:17]\n"
    "ds_read_b128 v[54:57], v243 offset:6144\n"
    "v_mfma_f32_16x16x32_bf16 v[18:21], a[68:71], v[58:61], v[18:21]\n"
    "buffer_load_dwordx4 v10, s[32:35], s53 offen lds\n"
    "ds_write_b128 v253, v[174:177] offset:1024\n"
    "v_mfma_f32_16x16x32_bf16 v[30:33], a[132:135], v[58:61], v[30:33]\n"
    "v_permlane32_swap_b32 v3, v5\n"
    "v_mfma_f32_16x16x32_bf16 v[14:17], a[8:11], v[82:85], v[14:17]\n"
    "ds_read_b128 v[58:61], v243 offset:7168\n"
    "v_mfma_f32_16x16x32_bf16 v[18:21], a[72:75], v[82:85], v[18:21]\n"
    "buffer_load_dwordx4 v11, s[32:35], s53 offen offset:1024 lds\n"
    "ds_write_b128 v253, v[178:181] offset:2048\n"
    "v_mfma_f32_16x16x32_bf16 v[30:33], a[136:139], v[82:85], v[30:33]\n"
    "v_add_f32 v2, v2, v4\n"
    "v_add_f32 v3, v3, v5\n"
    "v_mfma_f32_16x16x32_bf16 v[14:17], a[12:15], v[86:89], v[14:17]\n"
    "ds_read_b128 v[82:85], v243 offset:8192\n"
    "v_mfma_f32_16x16x32_bf16 v[18:21], a[76:79], v[86:89], v[18:21]\n"
    "buffer_load_dwordx4 v12, s[32:35], s53 offen offset:2048 lds\n"
    "ds_write_b128 v253, v[182:185] offset:3072\n"
    "v_mfma_f32_16x16x32_bf16 v[30:33], a[140:143], v[86:89], v[30:33]\n"
    "v_permlane16_swap_b32 v2, v3\n"
    "v_mfma_f32_16x16x32_bf16 v[14:17], a[16:19], v[90:93], v[14:17]\n"
    "ds_read_b128 v[86:89], v243 offset:9216\n"
    "v_mfma_f32_16x16x32_bf16 v[18:21], a[80:83], v[90:93], v[18:21]\n"
    "buffer_load_dwordx4 v13, s[32:35], s53 offen offset:3072 lds\n"
    "s_add_u32 s16, s53, 0x4000\n"
    "s_add_u32 m0, s80, 0xc000\n"
    "v_mfma_f32_16x16x32_bf16 v[30:33], a[144:147], v[90:93], v[30:33]\n"
    "v_add_f32 v2, v2, v3\n"
    "s_waitcnt lgkmcnt(3)\n"
    "v_mfma_f32_16x16x32_bf16 v[14:17], a[20:23], v[94:97], v[14:17]\n"
    "ds_read_b128 v[90:93], v243 offset:10240\n"
    "v_add_f32_dpp v2, v2, v2 row_ror:8 row_mask:0xf bank_mask:0xf\n"
    "v_mfma_f32_16x16x32_bf16 v[18:21], a[84:87], v[94:97], v[18:21]\n"
    "v_mov_b32 v3, v2\n"
    "s_nop 1\n"
    "v_permlane16_swap_b32 v2, v3\n"
    "v_mul_f32 v134, 0x3b000000, v2\n"
    "v_mul_f32 v136, v134, v134\n"
    "v_fma_f32 v136, v3, s76, -v136\n"
    "v_mfma_f32_16x16x32_bf16 v[114:117], a[176:179], v[22:25], v[114:117]\n"
    "buffer_load_dwordx4 v10, s[32:35], s16 offen lds\n"
    "s_waitcnt lgkmcnt(2)\n"
    "s_barrier\n"
    "v_mfma_f32_16x16x32_bf16 v[30:33], a[148:151], v[94:97], v[30:33]\n"
    "v_max_f32 v136, 0, v136\n"
    "v_add_f32 v135, s77, v136\n"
    "v_mfma_f32_16x16x32_bf16 v[14:17], a[24:27], v[54:57], v[14:17]\n"
    "ds_read_b128 v[94:97], v243 offset:11264\n"
    "v_mfma_f32_16x16x32_bf16 v[18:21], a[88:91], v[54:57], v[18:21]\n"
    "v_rsq_f32 v135, v135\n"
    "v_mfma_f32_16x16x32_bf16 v[30:33], a[152:155], v[54:57], v[30:33]\n"
    "buffer_load_dwordx4 v11, s[32:35], s16 offen offset:1024 lds\n"
    "v_mfma_f32_16x16x32_bf16 v[14:17], a[28:31], v[58:61], v[14:17]\n"
    "ds_read_b128 v[54:57], v243 offset:12288\n"
    "v_mul_f32 v126, 0xc0135761, v135\n"
    "v_mfma_f32_16x16x32_bf16 v[18:21], a[92:95], v[58:61], v[18:21]\n"
    "v_mul_f32 v136, v135, v135\n"
    "v_mfma_f32_16x16x32_bf16 v[30:33], a[156:159], v[58:61], v[30:33]\n"
    "v_rcp_f32 v128, v135\n"
    "v_mfma_f32_16x16x32_bf16 v[14:17], a[32:35], v[82:85], v[14:17]\n"
    "ds_read_b128 v[58:61], v243 offset:13312\n"
    "v_mul_f32 v136, 0x3d372713, v136\n"
    "v_mfma_f32_16x16x32_bf16 v[18:21], a[96:99], v[82:85], v[18:21]\n"
    "buffer_load_dwordx4 v12, s[32:35], s16 offen offset:2048 lds\n"
    "v_mfma_f32_16x16x32_bf16 v[30:33], a[160:163], v[82:85], v[30:33]\n"
    "v_sub_f32 v129, 0, v134\n"
    "s_waitcnt lgkmcnt(2)\n"
    "v_mfma_f32_16x16x32_bf16 v[14:17], a[36:39], v[86:89], v[14:17]\n"
    "ds_read_b128 v[82:85], v243 offset:14336\n"
    "v_mul_f32 v127, v136, v126\n"
    "v_mfma_f32_16x16x32_bf16 v[18:21], a[100:103], v[86:89], v[18:21]\n"
    "ds_write_b128 v242, v[126:129]\n"
    "buffer_load_dwordx4 v13, s[32:35], s16 offen offset:3072 lds\n"
    "v_subrev_u32 v12, 0xc000, v12\n"
    "v_subrev_u32 v13, 0xc000, v13\n"
    "v_mfma_f32_16x16x32_bf16 v[30:33], a[164:167], v[86:89], v[30:33]\n"
    "s_waitcnt vmcnt(16)\n"
    "ds_read_b128 v[170:173], v250\n"
    "v_mfma_f32_16x16x32_bf16 v[26:29], a[52:55], v[22:25], v[26:29]\n"
    "ds_read_b128 v[174:177], v250 offset:1024\n"
    "v_mfma_f32_16x16x32_bf16 v[34:37], a[116:119], v[22:25], v[34:37]\n"
    "ds_read_b128 v[178:181], v250 offset:2048\n"
    "v_mfma_f32_16x16x32_bf16 v[114:117], a[180:183], v[22:25], v[114:117]\n"
    "ds_read_b128 v[182:185], v250 offset:3072\n"
    "v_mfma_f32_16x16x32_bf16 v[26:29], a[56:59], v[22:25], v[26:29]\n"
    "ds_read_b64_tr_b16 v[66:67], v62 offset:16384\n"
    "ds_read_b64_tr_b16 v[68:69], v62 offset:18432\n"
    "ds_read_b64_tr_b16 v[70:71], v63 offset:16384\n"
    "ds_read_b64_tr_b16 v[72:73], v63 offset:18432\n"
    "ds_read_b64_tr_b16 v[74:75], v64 offset:16384\n"
    "ds_read_b64_tr_b16 v[76:77], v64 offset:18432\n"
    "s_waitcnt lgkmcnt(10)\n"
    "s_barrier\n"
    "v_mfma_f32_16x16x32_bf16 v[14:17], a[40:43], v[90:93], v[14:17]\n"
    "ds_read_b128 v[86:89], v243 offset:15360\n"
    "v_mfma_f32_16x16x32_bf16 v[18:21], a[104:107], v[90:93], v[18:21]\n"
    "ds_read_b128 v[138:141], v241\n"
    "v_mfma_f32_16x16x32_bf16 v[30:33], a[168:171], v[90:93], v[30:33]\n"
    "ds_read_b128 v[142:145], v241 offset:1024\n"
    "v_mfma_f32_16x16x32_bf16 v[14:17], a[44:47], v[94:97], v[14:17]\n"
    "ds_read_b128 v[90:93], v243 offset:16384\n"
    "v_mfma_f32_16x16x32_bf16 v[18:21], a[108:111], v[94:97], v[18:21]\n"
    "ds_read_b128 v[146:149], v241 offset:2048\n"
    "v_mfma_f32_16x16x32_bf16 v[30:33], a[172:175], v[94:97], v[30:33]\n"
    "ds_read_b128 v[150:153], v241 offset:3072\n"
    "v_mfma_f32_16x16x32_bf16 v[14:17], a[48:51], v[54:57], v[14:17]\n"
    "ds_read_b128 v[94:97], v243 offset:17408\n"
    "v_mfma_f32_16x16x32_bf16 v[18:21], a[112:115], v[54:57], v[18:21]\n"
    "s_waitcnt lgkmcnt(7)\n"
    "v_add_f32 v154, v154, v170\n"
    "v_mfma_f32_16x16x32_bf16 v[30:33], a[176:179], v[54:57], v[30:33]\n"
    "v_add_f32 v155, v155, v171\n"
    "v_add_f32 v156, v156, v172\n"
    "v_mfma_f32_16x16x32_bf16 v[14:17], a[52:55], v[58:61], v[14:17]\n"
    "ds_read_b128 v[54:57], v243 offset:18432\n"
    "v_add_f32 v157, v157, v173\n"
    "v_mfma_f32_16x16x32_bf16 v[18:21], a[116:119], v[58:61], v[18:21]\n"
    "v_add_f32 v158, v158, v174\n"
    "v_add_f32 v159, v159, v175\n"
    "v_mfma_f32_16x16x32_bf16 v[30:33], a[180:183], v[58:61], v[30:33]\n"
    "v_add_f32 v160, v160, v176\n"
    "v_add_f32 v161, v161, v177\n"
    "v_mfma_f32_16x16x32_bf16 v[14:17], a[56:59], v[82:85], v[14:17]\n"
    "ds_read_b128 v[58:61], v243 offset:19456\n"
    "v_add_f32 v162, v162, v178\n"
    "v_mfma_f32_16x16x32_bf16 v[18:21], a[120:123], v[82:85], v[18:21]\n"
    "v_add_f32 v163, v163, v179\n"
    "v_add_f32 v164, v164, v180\n"
    "v_mfma_f32_16x16x32_bf16 v[30:33], a[184:187], v[82:85], v[30:33]\n"
    "v_add_f32 v165, v165, v181\n"
    "s_waitcnt lgkmcnt(2)\n"
    "v_mfma_f32_16x16x32_bf16 v[14:17], a[60:63], v[86:89], v[14:17]\n"
    "ds_read_b128 v[82:85], v243 offset:20480\n"
    "v_add_f32 v166, v166, v182\n"
    "v_mfma_f32_16x16x32_bf16 v[18:21], a[124:127], v[86:89], v[18:21]\n"
    "v_add_f32 v167, v167, v183\n"
    "v_add_f32 v168, v168, v184\n"
    "v_mfma_f32_16x16x32_bf16 v[30:33], a[188:191], v[86:89], v[30:33]\n"
    "v_add_f32 v169, v169, v185\n"
    "v_cvt_pk_bf16_f32 v170, v154, v155\n"
    "v_cvt_pk_bf16_f32 v171, v156, v157\n"
    "v_cvt_pk_bf16_f32 v172, v158, v159\n"
    "v_cvt_pk_bf16_f32 v173, v160, v161\n"
    "v_cvt_pk_bf16_f32 v174, v162, v163\n"
    "v_cvt_pk_bf16_f32 v175, v164, v165\n"
    "v_cvt_pk_bf16_f32 v176, v166, v167\n"
    "v_cvt_pk_bf16_f32 v177, v168, v169\n"
    "v_permlane16_swap_b32 v170, v172\n"
    "v_permlane16_swap_b32 v171, v173\n"
    "v_permlane16_swap_b32 v174, v176\n"
    "v_permlane16_swap_b32 v175, v177\n"
    "v_mfma_f32_16x16x32_bf16 v[34:37], a[120:123], v[22:25], v[34:37]\n"
    "buffer_store_dwordx4 v[170:173], v245, s[36:39], s24 offen sc1\n"
    "ds_read_b64_tr_b16 v[78:79], v65 offset:16384\n"
    "ds_read_b64_tr_b16 v[80:81], v65 offset:18432\n"
    "s_add_u32 m0, s80, 0\n"
    "v_mfma_f32_16x16x32_bf16 v[114:117], a[184:187], v[22:25], v[114:117]\n"
    "ds_read_b64 v[186:187], v239\n"
    "v_pk_fma_f32 v[14:15], v[26:27], v[140:141], v[14:15] op_sel:[0,1,0] op_sel_hi:[1,1,1]\n"
    "v_pk_fma_f32 v[16:17], v[28:29], v[140:141], v[16:17] op_sel:[0,1,0] op_sel_hi:[1,1,1]\n"
    "v_pk_fma_f32 v[18:19], v[34:35], v[140:141], v[18:19] op_sel:[0,1,0] op_sel_hi:[1,1,1]\n"
    "v_pk_fma_f32 v[20:21], v[36:37], v[140:141], v[20:21] op_sel:[0,1,0] op_sel_hi:[1,1,1]\n"
    "buffer_store_dwordx4 v[174:177], v245, s[36:39], s24 offen offset:1024 sc1\n"
    "s_nop 1\n"
    "v_pk_fma_f32 v[30:31], v[114:115], v[140:141], v[30:31] op_sel:[0,1,0] op_sel_hi:[1,1,1]\n"
    "v_pk_fma_f32 v[32:33], v[116:117], v[140:141], v[32:33] op_sel:[0,1,0] op_sel_hi:[1,1,1]\n"
    "v_pk_mul_f32 v[98:99], v[14:15], v[14:15]\n"
    "v_pk_mul_f32 v[100:101], v[16:17], v[16:17]\n"
    "v_pk_mul_f32 v[102:103], v[18:19], v[18:19]\n"
    "v_pk_mul_f32 v[104:105], v[20:21], v[20:21]\n"
    "v_pk_mul_f32 v[118:119], v[30:31], v[30:31]\n"
    "v_pk_mul_f32 v[120:121], v[32:33], v[32:33]\n"
    "v_pk_fma_f32 v[98:99], v[98:99], v[138:139], v[138:139] op_sel:[0,1,0] op_sel_hi:[1,1,0]\n"
    "v_pk_fma_f32 v[100:101], v[100:101], v[138:139], v[138:139] op_sel:[0,1,0] op_sel_hi:[1,1,0]\n"
    "v_pk_fma_f32 v[102:103], v[102:103], v[138:139], v[138:139] op_sel:[0,1,0] op_sel_hi:[1,1,0]\n"
    "v_pk_fma_f32 v[104:105], v[104:105], v[138:139], v[138:139] op_sel:[0,1,0] op_sel_hi:[1,1,0]\n"
    "v_pk_fma_f32 v[118:119], v[118:119], v[138:139], v[138:139] op_sel:[0,1,0] op_sel_hi:[1,1,0]\n"
    "v_pk_fma_f32 v[120:121], v[120:121], v[138:139], v[138:139] op_sel:[0,1,0] op_sel_hi:[1,1,0]\n"
    "v_pk_mul_f32 v[98:99], v[98:99], v[14:15]\n"
    "v_pk_mul_f32 v[100:101], v[100:101], v[16:17]\n"
    "v_pk_mul_f32 v[102:103], v[102:103], v[18:19]\n"
    "v_pk_mul_f32 v[104:105], v[104:105], v[20:21]\n"
    "v_pk_mul_f32 v[118:119], v[118:119], v[30:31]\n"
    "v_pk_mul_f32 v[120:121], v[120:121], v[32:33]\n"
    "v_mfma_f32_16x16x32_bf16 v[38:41], a[0:3], v[90:93], 0\n"
    "buffer_load_dwordx4 v10, s[32:35], s48 offen lds\n"
    "ds_read_b128 v[86:89], v243 offset:21504\n"
    "v_mfma_f32_16x16x32_bf16 v[46:49], a[64:67], v[90:93], 0\n"
    "v_exp_f32 v98, v98\n"
    "v_mfma_f32_16x16x32_bf16 v[50:53], a[128:131], v[90:93], 0\n"
    "v_exp_f32 v99, v99\n"
    "v_mfma_f32_16x16x32_bf16 v[38:41], a[4:7], v[94:97], v[38:41]\n"
    "ds_read_b128 v[90:93], v243 offset:22528\n"
    "s_waitcnt lgkmcnt(2)\n"
    "v_mfma_f32_16x16x32_bf16 v[46:49], a[68:71], v[94:97], v[46:49]\n"
    "v_exp_f32 v100, v100\n"
    "v_mfma_f32_16x16x32_bf16 v[50:53], a[132:135], v[94:97], v[50:53]\n"
    "buffer_load_dwordx4 v11, s[32:35], s48 offen offset:1024 lds\n"
    "v_mfma_f32_16x16x32_bf16 v[38:41], a[8:11], v[54:57], v[38:41]\n"
    "ds_read_b128 v[94:97], v243 offset:23552\n"
    "v_fma_f32 v98, v98, v140, v140\n"
    "v_mfma_f32_16x16x32_bf16 v[46:49], a[72:75], v[54:57], v[46:49]\n"
    "v_exp_f32 v101, v101\n"
    "v_mfma_f32_16x16x32_bf16 v[50:53], a[136:139], v[54:57], v[50:53]\n"
    "v_exp_f32 v102, v102\n"
    "v_mfma_f32_16x16x32_bf16 v[38:41], a[12:15], v[58:61], v[38:41]\n"
    "ds_read_b128 v[54:57], v243 offset:24576\n"
    "v_exp_f32 v103, v103\n"
    "v_mfma_f32_16x16x32_bf16 v[46:49], a[76:79], v[58:61], v[46:49]\n"
    "buffer_load_dwordx4 v12, s[32:35], s48 offen offset:2048 lds\n"
    "v_mfma_f32_16x16x32_bf16 v[50:53], a[140:143], v[58:61], v[50:53]\n"
    "v_exp_f32 v104, v104\n"
    "v_mfma_f32_16x16x32_bf16 v[38:41], a[16:19], v[82:85], v[38:41]\n"
    "ds_read_b128 v[58:61], v243 offset:25600\n"
    "v_fma_f32 v99, v99, v140, v140\n"
    "v_mfma_f32_16x16x32_bf16 v[46:49], a[80:83], v[82:85], v[46:49]\n"
    "v_exp_f32 v105, v105\n"
    "v_mfma_f32_16x16x32_bf16 v[50:53], a[144:147], v[82:85], v[50:53]\n"
    "v_exp_f32 v118, v118\n"
    "s_waitcnt lgkmcnt(2)\n"
    "v_mfma_f32_16x16x32_bf16 v[38:41], a[20:23], v[86:89], v[38:41]\n"
    "buffer_load_dwordx4 v13, s[32:35], s48 offen offset:3072 lds\n"
    "s_add_u32 m0, s80, 0x4000\n"
    "ds_read_b128 v[82:85], v243 offset:26624\n"
    "v_mfma_f32_16x16x32_bf16 v[46:49], a[84:87], v[86:89], v[46:49]\n"
    "v_exp_f32 v119, v119\n"
    "v_mfma_f32_16x16x32_bf16 v[50:53], a[148:151], v[86:89], v[50:53]\n"
    "v_exp_f32 v120, v120\n"
    "v_mfma_f32_16x16x32_bf16 v[38:41], a[24:27], v[90:93], v[38:41]\n"
    "ds_read_b128 v[86:89], v243 offset:27648\n"
    "v_fma_f32 v100, v100, v140, v140\n"
    "v_mfma_f32_16x16x32_bf16 v[46:49], a[88:91], v[90:93], v[46:49]\n"
    "buffer_load_dwordx4 v10, s[32:35], s49 offen lds\n"
    "v_mfma_f32_16x16x32_bf16 v[50:53], a[152:155], v[90:93], v[50:53]\n"
    "v_exp_f32 v121, v121\n"
    "v_mfma_f32_16x16x32_bf16 v[38:41], a[28:31], v[94:97], v[38:41]\n"
    "ds_read_b128 v[90:93], v243 offset:28672\n"
    "v_fma_f32 v101, v101, v140, v140\n"
    "v_mfma_f32_16x16x32_bf16 v[46:49], a[92:95], v[94:97], v[46:49]\n"
    "v_rcp_f32 v98, v98\n"
    "v_mfma_f32_16x16x32_bf16 v[50:53], a[156:159], v[94:97], v[50:53]\n"
    "v_rcp_f32 v99, v99\n"
    "s_waitcnt lgkmcnt(2)\n"
    "v_mfma_f32_16x16x32_bf16 v[38:41], a[32:35], v[54:57], v[38:41]\n"
    "buffer_load_dwordx4 v11, s[32:35], s49 offen offset:1024 lds\n"
    "ds_read_b128 v[94:97], v243 offset:29696\n"
    "v_mfma_f32_16x16x32_bf16 v[46:49], a[96:99], v[54:57], v[46:49]\n"
    "v_rcp_f32 v100, v100\n"
    "v_mfma_f32_16x16x32_bf16 v[50:53], a[160:163], v[54:57], v[50:53]\n"
    "v_rcp_f32 v101, v101\n"
    "v_mfma_f32_16x16x32_bf16 v[38:41], a[36:39], v[58:61], v[38:41]\n"
    "ds_read_b128 v[54:57], v243 offset:30720\n"
    "v_fma_f32 v102, v102, v140, v140\n"
    "v_mfma_f32_16x16x32_bf16 v[46:49], a[100:103], v[58:61], v[46:49]\n"
    "v_rcp_f32 v102, v102\n"
    "v_mfma_f32_16x16x32_bf16 v[50:53], a[164:167], v[58:61], v[50:53]\n"
    "buffer_load_dwordx4 v12, s[32:35], s49 offen offset:2048 lds\n"
    "s_waitcnt vmcnt(21)\n"
    "s_barrier\n"
    "v_mfma_f32_16x16x32_bf16 v[38:41], a[40:43], v[82:85], v[38:41]\n"
    "ds_read_b128 v[58:61], v243 offset:31744\n"
    "v_fma_f32 v103, v103, v140, v140\n"
    "v_mfma_f32_16x16x32_bf16 v[46:49], a[104:107], v[82:85], v[46:49]\n"
    "v_rcp_f32 v103, v103\n"
    "v_mfma_f32_16x16x32_bf16 v[50:53], a[168:171], v[82:85], v[50:53]\n"
    "v_fma_f32 v104, v104, v140, v140\n"
    "s_waitcnt lgkmcnt(2)\n"
    "v_mfma_f32_16x16x32_bf16 v[38:41], a[44:47], v[86:89], v[38:41]\n"
    "buffer_load_dwordx4 v13, s[32:35], s49 offen offset:3072 lds\n"
    "s_add_u32 m0, s80, 0x10800\n"
    "ds_read_b128 v[82:85], v243 offset:32768\n"
    "v_mfma_f32_16x16x32_bf16 v[46:49], a[108:111], v[86:89], v[46:49]\n"
    "v_rcp_f32 v104, v104\n"
    "v_mfma_f32_16x16x32_bf16 v[50:53], a[172:175], v[86:89], v[50:53]\n"
    "v_fma_f32 v105, v105, v140, v140\n"
    "v_fma_f32 v118, v118, v140, v140\n"
    "v_mfma_f32_16x16x32_bf16 v[38:41], a[48:51], v[90:93], v[38:41]\n"
    "ds_read_b128 v[86:89], v243 offset:33792\n"
    "v_fma_f32 v119, v119, v140, v140\n"
    "v_mfma_f32_16x16x32_bf16 v[46:49], a[112:115], v[90:93], v[46:49]\n"
    "buffer_load_dwordx4 v10, s[32:35], s50 offen lds\n"
    "v_mfma_f32_16x16x32_bf16 v[50:53], a[176:179], v[90:93], v[50:53]\n"
    "v_rcp_f32 v105, v105\n"
    "v_mfma_f32_16x16x32_bf16 v[38:41], a[52:55], v[94:97], v[38:41]\n"
    "ds_read_b128 v[90:93], v243 offset:34816\n"
    "s_waitcnt vmcnt(9)\n"
    "v_mfma_f32_16x16x32_bf16 v[46:49], a[116:119], v[94:97], v[46:49]\n"
    "v_rcp_f32 v118, v118\n"
    "v_mfma_f32_16x16x32_bf16 v[50:53], a[180:183], v[94:97], v[50:53]\n"
    "buffer_store_dwordx2 v[222:223], v227, s[36:39], s21 offen sc1\n"
    "s_lshr_b32 s20, s8, 4\n"
    "s_lshl1_add_u32 s20, s6, s20\n"
    "s_lshl2_add_u32 s20, s20, s3\n"
    "s_lshl_b32 s20, s20, 7\n"
    "s_add_u32 s20, s20, 0x7000\n"
    "s_waitcnt lgkmcnt(1)\n"
    "v_mfma_f32_16x16x32_bf16 v[38:41], a[56:59], v[54:57], v[38:41]\n"
    "ds_read_b128 v[94:97], v243 offset:35840\n"
    "v_fma_f32 v120, v120, v140, v140\n"
    "v_mfma_f32_16x16x32_bf16 v[46:49], a[120:123], v[54:57], v[46:49]\n"
    "v_rcp_f32 v119, v119\n"
    "v_mfma_f32_16x16x32_bf16 v[50:53], a[184:187], v[54:57], v[50:53]\n"
    "buffer_load_dwordx4 v11, s[32:35], s50 offen offset:1024 lds\n"
    "v_mfma_f32_16x16x32_bf16 v[38:41], a[60:63], v[58:61], v[38:41]\n"
    "ds_read_b128 v[54:57], v243 offset:36864\n"
    "v_fma_f32 v121, v121, v140, v140\n"
    "v_mfma_f32_16x16x32_bf16 v[46:49], a[124:127], v[58:61], v[46:49]\n"
    "v_rcp_f32 v120, v120\n"
    "v_mfma_f32_16x16x32_bf16 v[50:53], a[188:191], v[58:61], v[50:53]\n"
    "ds_read_b64 v[188:189], v239 offset:16384\n"
    "v_rcp_f32 v121, v121\n"
    "v_lshlrev_b32 v22, 16, v186\n"
    "v_and_b32 v23, s78, v186\n"
    "v_pk_mul_f32 v[98:99], v[98:99], v[14:15]\n"
    "v_pk_mul_f32 v[100:101], v[100:101], v[16:17]\n"
    "v_pk_mul_f32 v[102:103], v[102:103], v[18:19]\n"
    "v_pk_mul_f32 v[104:105], v[104:105], v[20:21]\n"
    "v_cvt_pk_bf16_f32 v14, v98, v99\n"
    "buffer_load_dwordx4 v12, s[32:35], s50 offen offset:2048 lds\n"
    "v_cvt_pk_bf16_f32 v15, v100, v101\n"
    "v_cvt_pk_bf16_f32 v16, v102, v103\n"
    "v_cvt_pk_bf16_f32 v17, v104, v105\n"
    "v_pk_mul_f32 v[118:119], v[118:119], v[30:31]\n"
    "v_pk_mul_f32 v[120:121], v[120:121], v[32:33]\n"
    "v_cvt_pk_bf16_f32 v18, v118, v119\n"
    "v_cvt_pk_bf16_f32 v20, v118, v119\n"
    "v_cvt_pk_bf16_f32 v19, v120, v121\n"
    "v_cvt_pk_bf16_f32 v21, v120, v121\n"
    "v_permlane16_swap_b32 v18, v20\n"
    "v_lshlrev_b32 v24, 16, v187\n"
    "v_permlane16_swap_b32 v19, v21\n"
    "buffer_store_dwordx4 v[14:17], v252, s[36:39], s82 offen sc1\n"
    "s_add_u32 s82, s82, 0xac00\n"
    "v_pk_fma_f32 v[38:39], v[26:27], v[144:145], v[38:39] op_sel:[0,1,0] op_sel_hi:[1,1,1]\n"
    "v_pk_fma_f32 v[40:41], v[28:29], v[144:145], v[40:41] op_sel:[0,1,0] op_sel_hi:[1,1,1]\n"
    "v_pk_fma_f32 v[46:47], v[34:35], v[144:145], v[46:47] op_sel:[0,1,0] op_sel_hi:[1,1,1]\n"
    "v_pk_fma_f32 v[48:49], v[36:37], v[144:145], v[48:49] op_sel:[0,1,0] op_sel_hi:[1,1,1]\n"
    "v_pk_fma_f32 v[50:51], v[114:115], v[144:145], v[50:51] op_sel:[0,1,0] op_sel_hi:[1,1,1]\n"
    "v_pk_fma_f32 v[52:53], v[116:117], v[144:145], v[52:53] op_sel:[0,1,0] op_sel_hi:[1,1,1]\n"
    "v_pk_mul_f32 v[98:99], v[38:39], v[38:39]\n"
    "v_pk_mul_f32 v[100:101], v[40:41], v[40:41]\n"
    "v_pk_mul_f32 v[102:103], v[46:47], v[46:47]\n"
    "v_pk_mul_f32 v[104:105], v[48:49], v[48:49]\n"
    "v_pk_mul_f32 v[118:119], v[50:51], v[50:51]\n"
    "v_pk_mul_f32 v[120:121], v[52:53], v[52:53]\n"
    "v_pk_fma_f32 v[98:99], v[98:99], v[142:143], v[142:143] op_sel:[0,1,0] op_sel_hi:[1,1,0]\n"
    "v_pk_fma_f32 v[100:101], v[100:101], v[142:143], v[142:143] op_sel:[0,1,0] op_sel_hi:[1,1,0]\n"
    "v_pk_fma_f32 v[102:103], v[102:103], v[142:143], v[142:143] op_sel:[0,1,0] op_sel_hi:[1,1,0]\n"
    "v_pk_fma_f32 v[104:105], v[104:105], v[142:143], v[142:143] op_sel:[0,1,0] op_sel_hi:[1,1,0]\n"
    "buffer_store_dwordx4 v[18:21], v240, s[36:39], s83 offen sc1\n"
    "s_add_u32 s83, s83, 0xac00\n"
    "v_pk_fma_f32 v[118:119], v[118:119], v[142:143], v[142:143] op_sel:[0,1,0] op_sel_hi:[1,1,0]\n"
    "v_pk_fma_f32 v[120:121], v[120:121], v[142:143], v[142:143] op_sel:[0,1,0] op_sel_hi:[1,1,0]\n"
    "v_pk_mul_f32 v[98:99], v[98:99], v[38:39]\n"
    "v_pk_mul_f32 v[100:101], v[100:101], v[40:41]\n"
    "v_pk_mul_f32 v[102:103], v[102:103], v[46:47]\n"
    "v_pk_mul_f32 v[104:105], v[104:105], v[48:49]\n"
    "v_pk_mul_f32 v[118:119], v[118:119], v[50:51]\n"
    "v_pk_mul_f32 v[120:121], v[120:121], v[52:53]\n"
    "v_mfma_f32_16x16x32_bf16 v[14:17], a[0:3], v[82:85], 0\n"
    "ds_read_b128 v[58:61], v243 offset:37888\n"
    "buffer_load_dwordx4 v13, s[32:35], s50 offen offset:3072 lds\n"
    "v_mfma_f32_16x16x32_bf16 v[18:21], a[64:67], v[82:85], 0\n"
    "v_exp_f32 v98, v98\n"
    "v_mfma_f32_16x16x32_bf16 v[30:33], a[128:131], v[82:85], 0\n"
    "v_exp_f32 v99, v99\n"
    "v_mfma_f32_16x16x32_bf16 v[14:17], a[4:7], v[86:89], v[14:17]\n"
    "ds_read_b128 v[82:85], v243 offset:38912\n"
    "v_fma_f32 v98, v98, v144, v144\n"
    "v_mfma_f32_16x16x32_bf16 v[18:21], a[68:71], v[86:89], v[18:21]\n"
    "v_exp_f32 v100, v100\n"
    "v_mfma_f32_16x16x32_bf16 v[30:33], a[132:135], v[86:89], v[30:33]\n"
    "v_fma_f32 v99, v99, v144, v144\n"
    "s_waitcnt lgkmcnt(2)\n"
    "v_mfma_f32_16x16x32_bf16 v[14:17], a[8:11], v[90:93], v[14:17]\n"
    "ds_read_b128 v[86:89], v243 offset:39936\n"
    "v_fma_f32 v100, v100, v144, v144\n"
    "v_mfma_f32_16x16x32_bf16 v[18:21], a[72:75], v[90:93], v[18:21]\n"
    "v_exp_f32 v101, v101\n"
    "v_mfma_f32_16x16x32_bf16 v[30:33], a[136:139], v[90:93], v[30:33]\n"
    "v_exp_f32 v102, v102\n"
    "v_mfma_f32_16x16x32_bf16 v[14:17], a[12:15], v[94:97], v[14:17]\n"
    "ds_read_b128 v[90:93], v243 offset:40960\n"
    "v_fma_f32 v101, v101, v144, v144\n"
    "v_mfma_f32_16x16x32_bf16 v[18:21], a[76:79], v[94:97], v[18:21]\n"
    "v_exp_f32 v103, v103\n"
    "v_mfma_f32_16x16x32_bf16 v[30:33], a[140:143], v[94:97], v[30:33]\n"
    "v_exp_f32 v104, v104\n"
    "v_mfma_f32_16x16x32_bf16 v[14:17], a[16:19], v[54:57], v[14:17]\n"
    "ds_read_b128 v[94:97], v243 offset:41984\n"
    "v_fma_f32 v102, v102, v144, v144\n"
    "v_mfma_f32_16x16x32_bf16 v[18:21], a[80:83], v[54:57], v[18:21]\n"
    "v_exp_f32 v105, v105\n"
    "v_mfma_f32_16x16x32_bf16 v[30:33], a[144:147], v[54:57], v[30:33]\n"
    "v_fma_f32 v103, v103, v144, v144\n"
    "s_waitcnt lgkmcnt(2)\n"
    "v_mfma_f32_16x16x32_bf16 v[14:17], a[20:23], v[58:61], v[14:17]\n"
    "ds_read_b128 v[54:57], v243 offset:43008\n"
    "v_fma_f32 v104, v104, v144, v144\n"
    "v_mfma_f32_16x16x32_bf16 v[18:21], a[84:87], v[58:61], v[18:21]\n"
    "v_exp_f32 v118, v118\n"
    "v_mfma_f32_16x16x32_bf16 v[30:33], a[148:151], v[58:61], v[30:33]\n"
    "v_exp_f32 v119, v119\n"
    "v_mfma_f32_16x16x32_bf16 v[14:17], a[24:27], v[82:85], v[14:17]\n"
    "ds_read_b128 v[58:61], v243 offset:44032\n"
    "v_fma_f32 v105, v105, v144, v144\n"
    "v_mfma_f32_16x16x32_bf16 v[18:21], a[88:91], v[82:85], v[18:21]\n"
    "v_exp_f32 v120, v120\n"
    "v_mfma_f32_16x16x32_bf16 v[30:33], a[152:155], v[82:85], v[30:33]\n"
    "v_exp_f32 v121, v121\n"
    "v_mfma_f32_16x16x32_bf16 v[14:17], a[28:31], v[86:89], v[14:17]\n"
    "ds_read_b128 v[82:85], v243 offset:45056\n"
    "v_fma_f32 v118, v118, v144, v144\n"
    "v_mfma_f32_16x16x32_bf16 v[18:21], a[92:95], v[86:89], v[18:21]\n"
    "v_rcp_f32 v98, v98\n"
    "v_mfma_f32_16x16x32_bf16 v[30:33], a[156:159], v[86:89], v[30:33]\n"
    "v_fma_f32 v119, v119, v144, v144\n"
    "s_waitcnt lgkmcnt(2)\n"
    "v_mfma_f32_16x16x32_bf16 v[14:17], a[32:35], v[90:93], v[14:17]\n"
    "ds_read_b128 v[86:89], v243 offset:46080\n"
    "v_fma_f32 v120, v120, v144, v144\n"
    "v_mfma_f32_16x16x32_bf16 v[18:21], a[96:99], v[90:93], v[18:21]\n"
    "v_rcp_f32 v99, v99\n"
    "v_mfma_f32_16x16x32_bf16 v[30:33], a[160:163], v[90:93], v[30:33]\n"
    "v_rcp_f32 v100, v100\n"
    "v_mfma_f32_16x16x32_bf16 v[14:17], a[36:39], v[94:97], v[14:17]\n"
    "ds_read_b128 v[90:93], v243 offset:47104\n"
    "v_fma_f32 v121, v121, v144, v144\n"
    "v_mfma_f32_16x16x32_bf16 v[18:21], a[100:103], v[94:97], v[18:21]\n"
    "v_rcp_f32 v101, v101\n"
    "v_mfma_f32_16x16x32_bf16 v[30:33], a[164:167], v[94:97], v[30:33]\n"
    "s_barrier\n"
    "v_mfma_f32_16x16x32_bf16 v[14:17], a[40:43], v[54:57], v[14:17]\n"
    "ds_read_b128 v[94:97], v243 offset:48128\n"
    "v_and_b32 v25, s78, v187\n"
    "v_mfma_f32_16x16x32_bf16 v[18:21], a[104:107], v[54:57], v[18:21]\n"
    "v_rcp_f32 v102, v102\n"
    "v_mfma_f32_16x16x32_bf16 v[30:33], a[168:171], v[54:57], v[30:33]\n"
    "v_lshlrev_b32 v210, 16, v188\n"
    "s_waitcnt lgkmcnt(2)\n"
    "v_mfma_f32_16x16x32_bf16 v[14:17], a[44:47], v[58:61], v[14:17]\n"
    "ds_read_b128 v[54:57], v243 offset:49152\n"
    "v_and_b32 v211, s78, v188\n"
    "v_mfma_f32_16x16x32_bf16 v[18:21], a[108:111], v[58:61], v[18:21]\n"
    "v_rcp_f32 v103, v103\n"
    "v_mfma_f32_16x16x32_bf16 v[30:33], a[172:175], v[58:61], v[30:33]\n"
    "v_rcp_f32 v104, v104\n"
    "v_mfma_f32_16x16x32_bf16 v[14:17], a[48:51], v[82:85], v[14:17]\n"
    "ds_read_b128 v[58:61], v243 offset:50176\n"
    "v_lshlrev_b32 v212, 16, v189\n"
    "v_mfma_f32_16x16x32_bf16 v[18:21], a[112:115], v[82:85], v[18:21]\n"
    "v_rcp_f32 v105, v105\n"
    "v_mfma_f32_16x16x32_bf16 v[30:33], a[176:179], v[82:85], v[30:33]\n"
    "v_rcp_f32 v118, v118\n"
    "v_mfma_f32_16x16x32_bf16 v[14:17], a[52:55], v[86:89], v[14:17]\n"
    "ds_read_b128 v[82:85], v243 offset:51200\n"
    "v_and_b32 v213, s78, v189\n"
    "v_mfma_f32_16x16x32_bf16 v[18:21], a[116:119], v[86:89], v[18:21]\n"
    "v_rcp_f32 v119, v119\n"
    "v_mfma_f32_16x16x32_bf16 v[30:33], a[180:183], v[86:89], v[30:33]\n"
    "s_waitcnt lgkmcnt(2)\n"
    "v_mfma_f32_16x16x32_bf16 v[14:17], a[56:59], v[90:93], v[14:17]\n"
    "ds_read_b128 v[86:89], v243 offset:52224\n"
    "v_mfma_f32_16x16x32_bf16 v[18:21], a[120:123], v[90:93], v[18:21]\n"
    "v_rcp_f32 v120, v120\n"
    "v_mfma_f32_16x16x32_bf16 v[30:33], a[184:187], v[90:93], v[30:33]\n"
    "v_rcp_f32 v121, v121\n"
    "v_mfma_f32_16x16x32_bf16 v[14:17], a[60:63], v[94:97], v[14:17]\n"
    "ds_read_b128 v[90:93], v243 offset:53248\n"
    "v_mfma_f32_16x16x32_bf16 v[18:21], a[124:127], v[94:97], v[18:21]\n"
    "v_mfma_f32_16x16x32_bf16 v[30:33], a[188:191], v[94:97], v[30:33]\n"
    "ds_read_b64 v[190:191], v239 offset:32768\n"
    "v_pk_mul_f32 v[98:99], v[98:99], v[38:39]\n"
    "v_pk_mul_f32 v[100:101], v[100:101], v[40:41]\n"
    "v_pk_mul_f32 v[102:103], v[102:103], v[46:47]\n"
    "v_pk_mul_f32 v[104:105], v[104:105], v[48:49]\n"
    "v_cvt_pk_bf16_f32 v38, v98, v99\n"
    "v_cvt_pk_bf16_f32 v39, v100, v101\n"
    "v_cvt_pk_bf16_f32 v40, v102, v103\n"
    "v_cvt_pk_bf16_f32 v41, v104, v105\n"
    "buffer_store_dwordx4 v[38:41], v252, s[36:39], s82 offen sc1\n"
    "s_add_u32 s82, s82, 0xac00\n"
    "v_pk_mul_f32 v[118:119], v[118:119], v[50:51]\n"
    "v_pk_mul_f32 v[120:121], v[120:121], v[52:53]\n"
    "v_cvt_pk_bf16_f32 v46, v118, v119\n"
    "v_cvt_pk_bf16_f32 v48, v118, v119\n"
    "v_cvt_pk_bf16_f32 v47, v120, v121\n"
    "v_cvt_pk_bf16_f32 v49, v120, v121\n"
    "v_permlane16_swap_b32 v46, v48\n"
    "s_nop 0\n"
    "v_permlane16_swap_b32 v47, v49\n"
    "v_pk_fma_f32 v[14:15], v[26:27], v[148:149], v[14:15] op_sel:[0,1,0] op_sel_hi:[1,1,1]\n"
    "v_pk_fma_f32 v[16:17], v[28:29], v[148:149], v[16:17] op_sel:[0,1,0] op_sel_hi:[1,1,1]\n"
    "v_pk_fma_f32 v[18:19], v[34:35], v[148:149], v[18:19] op_sel:[0,1,0] op_sel_hi:[1,1,1]\n"
    "v_pk_fma_f32 v[20:21], v[36:37], v[148:149], v[20:21] op_sel:[0,1,0] op_sel_hi:[1,1,1]\n"
    "v_pk_fma_f32 v[30:31], v[114:115], v[148:149], v[30:31] op_sel:[0,1,0] op_sel_hi:[1,1,1]\n"
    "buffer_store_dwordx4 v[46:49], v240, s[36:39], s83 offen sc1\n"
    "s_add_u32 s83, s83, 0xac00\n"
    "v_pk_fma_f32 v[32:33], v[116:117], v[148:149], v[32:33] op_sel:[0,1,0] op_sel_hi:[1,1,1]\n"
    "v_pk_mul_f32 v[98:99], v[14:15], v[14:15]\n"
    "v_pk_mul_f32 v[100:101], v[16:17], v[16:17]\n"
    "v_pk_mul_f32 v[102:103], v[18:19], v[18:19]\n"
    "v_pk_mul_f32 v[104:105], v[20:21], v[20:21]\n"
    "v_pk_mul_f32 v[118:119], v[30:31], v[30:31]\n"
    "v_pk_mul_f32 v[120:121], v[32:33], v[32:33]\n"
    "v_pk_fma_f32 v[98:99], v[98:99], v[146:147], v[146:147] op_sel:[0,1,0] op_sel_hi:[1,1,0]\n"
    "v_pk_fma_f32 v[100:101], v[100:101], v[146:147], v[146:147] op_sel:[0,1,0] op_sel_hi:[1,1,0]\n"
    "v_pk_fma_f32 v[102:103], v[102:103], v[146:147], v[146:147] op_sel:[0,1,0] op_sel_hi:[1,1,0]\n"
    "v_pk_fma_f32 v[104:105], v[104:105], v[146:147], v[146:147] op_sel:[0,1,0] op_sel_hi:[1,1,0]\n"
    "v_pk_fma_f32 v[118:119], v[118:119], v[146:147], v[146:147] op_sel:[0,1,0] op_sel_hi:[1,1,0]\n"
    "v_pk_fma_f32 v[120:121], v[120:121], v[146:147], v[146:147] op_sel:[0,1,0] op_sel_hi:[1,1,0]\n"
    "v_pk_mul_f32 v[98:99], v[98:99], v[14:15]\n"
    "v_pk_mul_f32 v[100:101], v[100:101], v[16:17]\n"
    "v_pk_mul_f32 v[102:103], v[102:103], v[18:19]\n"
    "v_pk_mul_f32 v[104:105], v[104:105], v[20:21]\n"
    "v_pk_mul_f32 v[118:119], v[118:119], v[30:31]\n"
    "v_pk_mul_f32 v[120:121], v[120:121], v[32:33]\n"
    "v_mfma_f32_16x16x32_bf16 v[38:41], a[0:3], v[54:57], 0\n"
    "ds_read_b128 v[94:97], v243 offset:54272\n"
    "v_mfma_f32_16x16x32_bf16 v[46:49], a[64:67], v[54:57], 0\n"
    "v_exp_f32 v98, v98\n"
    "v_mfma_f32_16x16x32_bf16 v[50:53], a[128:131], v[54:57], 0\n"
    "v_fma_f32 v98, v98, v148, v148\n"
    "s_waitcnt lgkmcnt(1)\n"
    "v_mfma_f32_16x16x32_bf16 v[38:41], a[4:7], v[58:61], v[38:41]\n"
    "ds_read_b128 v[54:57], v243 offset:55296\n"
    "v_mfma_f32_16x16x32_bf16 v[46:49], a[68:71], v[58:61], v[46:49]\n"
    "v_exp_f32 v99, v99\n"
    "v_mfma_f32_16x16x32_bf16 v[50:53], a[132:135], v[58:61], v[50:53]\n"
    "v_exp_f32 v100, v100\n"
    "v_mfma_f32_16x16x32_bf16 v[38:41], a[8:11], v[82:85], v[38:41]\n"
    "ds_read_b128 v[58:61], v243 offset:56320\n"
    "v_fma_f32 v99, v99, v148, v148\n"
    "v_mfma_f32_16x16x32_bf16 v[46:49], a[72:75], v[82:85], v[46:49]\n"
    "v_exp_f32 v101, v101\n"
    "v_mfma_f32_16x16x32_bf16 v[50:53], a[136:139], v[82:85], v[50:53]\n"
    "v_exp_f32 v102, v102\n"
    "v_mfma_f32_16x16x32_bf16 v[38:41], a[12:15], v[86:89], v[38:41]\n"
    "ds_read_b128 v[82:85], v243 offset:57344\n"
    "v_fma_f32 v100, v100, v148, v148\n"
    "v_mfma_f32_16x16x32_bf16 v[46:49], a[76:79], v[86:89], v[46:49]\n"
    "v_exp_f32 v103, v103\n"
    "v_mfma_f32_16x16x32_bf16 v[50:53], a[140:143], v[86:89], v[50:53]\n"
    "buffer_load_dwordx2 v[136:137], v229, s[36:39], s20 offen sc1\n"
    "v_mfma_f32_16x16x32_bf16 v[38:41], a[16:19], v[90:93], v[38:41]\n"
    "ds_read_b128 v[86:89], v243 offset:58368\n"
    "v_fma_f32 v101, v101, v148, v148\n"
    "v_mfma_f32_16x16x32_bf16 v[46:49], a[80:83], v[90:93], v[46:49]\n"
    "v_exp_f32 v104, v104\n"
    "v_mfma_f32_16x16x32_bf16 v[50:53], a[144:147], v[90:93], v[50:53]\n"
    "v_fma_f32 v102, v102, v148, v148\n"
    "s_waitcnt lgkmcnt(2)\n"
    "v_mfma_f32_16x16x32_bf16 v[38:41], a[20:23], v[94:97], v[38:41]\n"
    "buffer_load_dwordx4 v[154:157], v252, s[36:39], s57 offen sc1\n"
    "ds_read_b128 v[90:93], v243 offset:59392\n"
    "v_mfma_f32_16x16x32_bf16 v[46:49], a[84:87], v[94:97], v[46:49]\n"
    "v_exp_f32 v105, v105\n"
    "v_mfma_f32_16x16x32_bf16 v[50:53], a[148:151], v[94:97], v[50:53]\n"
    "v_exp_f32 v118, v118\n"
    "v_mfma_f32_16x16x32_bf16 v[38:41], a[24:27], v[54:57], v[38:41]\n"
    "ds_read_b128 v[94:97], v243 offset:60416\n"
    "v_fma_f32 v103, v103, v148, v148\n"
    "v_mfma_f32_16x16x32_bf16 v[46:49], a[88:91], v[54:57], v[46:49]\n"
    "buffer_load_dwordx4 v[158:161], v252, s[36:39], s57 offen offset:1024 sc1\n"
    "v_mfma_f32_16x16x32_bf16 v[50:53], a[152:155], v[54:57], v[50:53]\n"
    "v_exp_f32 v119, v119\n"
    "v_mfma_f32_16x16x32_bf16 v[38:41], a[28:31], v[58:61], v[38:41]\n"
    "ds_read_b128 v[54:57], v243 offset:61440\n"
    "v_fma_f32 v104, v104, v148, v148\n"
    "v_mfma_f32_16x16x32_bf16 v[46:49], a[92:95], v[58:61], v[46:49]\n"
    "v_exp_f32 v120, v120\n"
    "v_mfma_f32_16x16x32_bf16 v[50:53], a[156:159], v[58:61], v[50:53]\n"
    "buffer_load_dwordx4 v[162:165], v252, s[36:39], s84 offen sc1\n"
    "s_waitcnt lgkmcnt(2)\n"
    "v_mfma_f32_16x16x32_bf16 v[38:41], a[32:35], v[82:85], v[38:41]\n"
    "ds_read_b128 v[58:61], v243 offset:62464\n"
    "v_fma_f32 v105, v105, v148, v148\n"
    "v_mfma_f32_16x16x32_bf16 v[46:49], a[96:99], v[82:85], v[46:49]\n"
    "v_exp_f32 v121, v121\n"
    "v_mfma_f32_16x16x32_bf16 v[50:53], a[160:163], v[82:85], v[50:53]\n"
    "v_rcp_f32 v98, v98\n"
    "v_mfma_f32_16x16x32_bf16 v[38:41], a[36:39], v[86:89], v[38:41]\n"
    "ds_read_b128 v[82:85], v243 offset:63488\n"
    "buffer_load_dwordx4 v[166:169], v252, s[36:39], s84 offen offset:1024 sc1\n"
    "v_mfma_f32_16x16x32_bf16 v[46:49], a[100:103], v[86:89], v[46:49]\n"
    "v_rcp_f32 v99, v99\n"
    "v_mfma_f32_16x16x32_bf16 v[50:53], a[164:167], v[86:89], v[50:53]\n"
    "v_rcp_f32 v100, v100\n"
    "v_mfma_f32_16x16x32_bf16 v[38:41], a[40:43], v[90:93], v[38:41]\n"
    "ds_read_b128 v[86:89], v243 offset:64512\n"
    "v_fma_f32 v118, v118, v148, v148\n"
    "v_mfma_f32_16x16x32_bf16 v[46:49], a[104:107], v[90:93], v[46:49]\n"
    "buffer_load_dwordx4 v[170:173], v252, s[36:39], s91 offen sc1\n"
    "v_mfma_f32_16x16x32_bf16 v[50:53], a[168:171], v[90:93], v[50:53]\n"
    "v_fma_f32 v119, v119, v148, v148\n"
    "s_waitcnt lgkmcnt(2)\n"
    "v_mfma_f32_16x16x32_bf16 v[38:41], a[44:47], v[94:97], v[38:41]\n"
    "v_rcp_f32 v101, v101\n"
    "v_mfma_f32_16x16x32_bf16 v[46:49], a[108:111], v[94:97], v[46:49]\n"
    "v_rcp_f32 v102, v102\n"
    "v_mfma_f32_16x16x32_bf16 v[50:53], a[172:175], v[94:97], v[50:53]\n"
    "ds_read_b64_tr_b16 a[160:161], v63 offset:32768\n"
    "buffer_load_dwordx4 v[174:177], v252, s[36:39], s91 offen offset:1024 sc1\n"
    "v_mfma_f32_16x16x32_bf16 v[38:41], a[48:51], v[54:57], v[38:41]\n"
    "v_rcp_f32 v103, v103\n"
    "v_mfma_f32_16x16x32_bf16 v[46:49], a[112:115], v[54:57], v[46:49]\n"
    "v_rcp_f32 v104, v104\n"
    "v_mfma_f32_16x16x32_bf16 v[50:53], a[176:179], v[54:57], v[50:53]\n"
    "v_rcp_f32 v105, v105\n"
    "v_mfma_f32_16x16x32_bf16 v[38:41], a[52:55], v[58:61], v[38:41]\n"
    "ds_read_b64_tr_b16 a[162:163], v63 offset:34816\n"
    "buffer_load_dwordx4 v[178:181], v252, s[36:39], s94 offen sc1\n"
    "v_mfma_f32_16x16x32_bf16 v[46:49], a[116:119], v[58:61], v[46:49]\n"
    "v_rcp_f32 v118, v118\n"
    "v_mfma_f32_16x16x32_bf16 v[50:53], a[180:183], v[58:61], v[50:53]\n"
    "v_fma_f32 v120, v120, v148, v148\n"
    "s_waitcnt lgkmcnt(2)\n"
    "v_mfma_f32_16x16x32_bf16 v[38:41], a[56:59], v[82:85], v[38:41]\n"
    "v_rcp_f32 v119, v119\n"
    "v_mfma_f32_16x16x32_bf16 v[46:49], a[120:123], v[82:85], v[46:49]\n"
    "ds_read_b64_tr_b16 a[36:37], v64 offset:32768\n"
    "buffer_load_dwordx4 v[182:185], v252, s[36:39], s94 offen offset:1024 sc1\n"
    "s_add_u32 m0, s80, 0x21000\n"
    "v_mfma_f32_16x16x32_bf16 v[50:53], a[184:187], v[82:85], v[50:53]\n"
    "v_rcp_f32 v120, v120\n"
    "v_mfma_f32_16x16x32_bf16 v[38:41], a[60:63], v[86:89], v[38:41]\n"
    "v_fma_f32 v121, v121, v148, v148\n"
    "v_mfma_f32_16x16x32_bf16 v[46:49], a[124:127], v[86:89], v[46:49]\n"
    "v_rcp_f32 v121, v121\n"
    "v_mfma_f32_16x16x32_bf16 v[50:53], a[188:191], v[86:89], v[50:53]\n"
    "ds_read_b64 v[192:193], v239 offset:49152\n"
    "buffer_load_dwordx4 v252, s[36:39], s86 offen sc1 lds\n"
    "ds_read_b64_tr_b16 a[38:39], v64 offset:34816\n"
    "v_pk_mul_f32 v[98:99], v[98:99], v[14:15]\n"
    "v_pk_mul_f32 v[100:101], v[100:101], v[16:17]\n"
    "v_pk_mul_f32 v[102:103], v[102:103], v[18:19]\n"
    "v_pk_mul_f32 v[104:105], v[104:105], v[20:21]\n"
    "v_cvt_pk_bf16_f32 v14, v98, v99\n"
    "v_cvt_pk_bf16_f32 v15, v100, v101\n"
    "v_cvt_pk_bf16_f32 v16, v102, v103\n"
    "v_cvt_pk_bf16_f32 v17, v104, v105\n"
    "v_pk_mul_f32 v[118:119], v[118:119], v[30:31]\n"
    "v_pk_mul_f32 v[120:121], v[120:121], v[32:33]\n"
    "v_cvt_pk_bf16_f32 v18, v118, v119\n"
    "v_cvt_pk_bf16_f32 v20, v118, v119\n"
    "buffer_store_dwordx4 v[14:17], v252, s[36:39], s82 offen sc1\n"
    "s_add_u32 s82, s82, 0xac00\n"
    "v_cvt_pk_bf16_f32 v19, v120, v121\n"
    "v_cvt_pk_bf16_f32 v21, v120, v121\n"
    "v_permlane16_swap_b32 v18, v20\n"
    "s_nop 0\n"
    "v_permlane16_swap_b32 v19, v21\n"
    "v_pk_fma_f32 v[38:39], v[26:27], v[152:153], v[38:39] op_sel:[0,1,0] op_sel_hi:[1,1,1]\n"
    "v_pk_fma_f32 v[40:41], v[28:29], v[152:153], v[40:41] op_sel:[0,1,0] op_sel_hi:[1,1,1]\n"
    "v_pk_fma_f32 v[46:47], v[34:35], v[152:153], v[46:47] op_sel:[0,1,0] op_sel_hi:[1,1,1]\n"
    "v_pk_fma_f32 v[48:49], v[36:37], v[152:153], v[48:49] op_sel:[0,1,0] op_sel_hi:[1,1,1]\n"
    "v_pk_fma_f32 v[50:51], v[114:115], v[152:153], v[50:51] op_sel:[0,1,0] op_sel_hi:[1,1,1]\n"
    "v_pk_fma_f32 v[52:53], v[116:117], v[152:153], v[52:53] op_sel:[0,1,0] op_sel_hi:[1,1,1]\n"
    "v_pk_mul_f32 v[98:99], v[38:39], v[38:39]\n"
    "v_pk_mul_f32 v[100:101], v[40:41], v[40:41]\n"
    "v_pk_mul_f32 v[102:103], v[46:47], v[46:47]\n"
    "buffer_store_dwordx4 v[18:21], v240, s[36:39], s83 offen sc1\n"
    "s_add_u32 s83, s83, 0xac00\n"
    "v_pk_mul_f32 v[104:105], v[48:49], v[48:49]\n"
    "v_pk_mul_f32 v[118:119], v[50:51], v[50:51]\n"
    "v_pk_mul_f32 v[120:121], v[52:53], v[52:53]\n"
    "v_pk_fma_f32 v[98:99], v[98:99], v[150:151], v[150:151] op_sel:[0,1,0] op_sel_hi:[1,1,0]\n"
    "v_pk_fma_f32 v[100:101], v[100:101], v[150:151], v[150:151] op_sel:[0,1,0] op_sel_hi:[1,1,0]\n"
    "v_pk_fma_f32 v[102:103], v[102:103], v[150:151], v[150:151] op_sel:[0,1,0] op_sel_hi:[1,1,0]\n"
    "v_pk_fma_f32 v[104:105], v[104:105], v[150:151], v[150:151] op_sel:[0,1,0] op_sel_hi:[1,1,0]\n"
    "v_pk_fma_f32 v[118:119], v[118:119], v[150:151], v[150:151] op_sel:[0,1,0] op_sel_hi:[1,1,0]\n"
    "v_pk_fma_f32 v[120:121], v[120:121], v[150:151], v[150:151] op_sel:[0,1,0] op_sel_hi:[1,1,0]\n"
    "v_pk_mul_f32 v[98:99], v[98:99], v[38:39]\n"
    "v_pk_mul_f32 v[100:101], v[100:101], v[40:41]\n"
    "v_pk_mul_f32 v[102:103], v[102:103], v[46:47]\n"
    "v_pk_mul_f32 v[104:105], v[104:105], v[48:49]\n"
    "v_pk_mul_f32 v[118:119], v[118:119], v[50:51]\n"
    "v_pk_mul_f32 v[120:121], v[120:121], v[52:53]\n"
    "buffer_load_dwordx4 v252, s[36:39], s86 offen offset:1024 sc1 lds\n"
    "s_add_u32 m0, s80, 0x21800\n"
    "v_exp_f32 v98, v98\n"
    "v_exp_f32 v99, v99\n"
    "v_exp_f32 v100, v100\n"
    "v_exp_f32 v101, v101\n"
    "v_exp_f32 v102, v102\n"
    "v_exp_f32 v103, v103\n"
    "v_exp_f32 v104, v104\n"
    "v_exp_f32 v105, v105\n"
    "v_exp_f32 v118, v118\n"
    "buffer_load_dwordx4 v252, s[36:39], s90 offen sc1 lds\n"
    "s_waitcnt vmcnt(13)\n"
    "v_cmp_ne_u64 vcc, s[88:89], v[136:137]\n"
    "v_exp_f32 v119, v119\n"
    "s_nop 0\n"
    "s_cbranch_vccnz .Lspin_10_%=\n"
    ".Lback_11_%=:\n"
    "s_mov_b32 m0, s59\n"
    "v_exp_f32 v120, v120\n"
    "v_exp_f32 v121, v121\n"
    "v_pk_fma_f32 v[98:99], v[98:99], v[152:153], v[152:153] op_sel:[0,0,0] op_sel_hi:[1,0,0]\n"
    "v_pk_fma_f32 v[100:101], v[100:101], v[152:153], v[152:153] op_sel:[0,0,0] op_sel_hi:[1,0,0]\n"
    "v_pk_fma_f32 v[102:103], v[102:103], v[152:153], v[152:153] op_sel:[0,0,0] op_sel_hi:[1,0,0]\n"
    "v_pk_fma_f32 v[104:105], v[104:105], v[152:153], v[152:153] op_sel:[0,0,0] op_sel_hi:[1,0,0]\n"
    "v_pk_fma_f32 v[118:119], v[118:119], v[152:153], v[152:153] op_sel:[0,0,0] op_sel_hi:[1,0,0]\n"
    "v_pk_fma_f32 v[120:121], v[120:121], v[152:153], v[152:153] op_sel:[0,0,0] op_sel_hi:[1,0,0]\n"
    "v_rcp_f32 v98, v98\n"
    "v_rcp_f32 v99, v99\n"
    "buffer_load_dwordx4 v216, s[36:39], s58 offen sc1 lds\n"
    "v_rcp_f32 v100, v100\n"
    "v_rcp_f32 v101, v101\n"
    "v_rcp_f32 v102, v102\n"
    "v_rcp_f32 v103, v103\n"
    "v_rcp_f32 v104, v104\n"
    "v_rcp_f32 v105, v105\n"
    "v_rcp_f32 v118, v118\n"
    "v_rcp_f32 v119, v119\n"
    "v_rcp_f32 v120, v120\n"
    "v_rcp_f32 v121, v121\n"
    "buffer_load_dwordx4 v217, s[36:39], s58 offen offset:1024 sc1 lds\n"
    "v_add_u32 v247, 0xac00, v252\n"
    "v_pk_mul_f32 v[98:99], v[98:99], v[38:39]\n"
    "v_pk_mul_f32 v[100:101], v[100:101], v[40:41]\n"
    "v_pk_mul_f32 v[102:103], v[102:103], v[46:47]\n"
    "v_pk_mul_f32 v[104:105], v[104:105], v[48:49]\n"
    "v_pk_mul_f32 v[118:119], v[118:119], v[50:51]\n"
    "v_pk_mul_f32 v[120:121], v[120:121], v[52:53]\n"
    "v_cvt_pk_bf16_f32 v38, v98, v99\n"
    "v_cvt_pk_bf16_f32 v39, v100, v101\n"
    "v_cvt_pk_bf16_f32 v40, v102, v103\n"
    "v_cvt_pk_bf16_f32 v41, v104, v105\n"
    "v_cvt_pk_bf16_f32 v46, v118, v119\n"
    "v_cvt_pk_bf16_f32 v48, v118, v119\n"
    "v_cvt_pk_bf16_f32 v47, v120, v121\n"
    "s_or_b32 s18, %1, s3\n"
    "s_cmp_eq_u32 s18, 0\n"
    "buffer_store_dwordx4 v[38:41], v252, s[36:39], s82 offen sc1\n"
    "v_cvt_pk_bf16_f32 v49, v120, v121\n"
    "v_permlane16_swap_b32 v46, v48\n"
    "s_cselect_b32 s18, 0, 0x3f000000\n"
    "v_permlane16_swap_b32 v47, v49\n"
    "s_waitcnt lgkmcnt(0)\n"
    "s_barrier\n"
    "s_waitcnt vmcnt(15)\n"
    "v_mfma_f32_16x16x32_bf16 v[98:101], a[196:199], v[154:157], 0\n"
    "ds_read_b64_tr_b16 a[100:101], v65 offset:32768\n"
    "v_mfma_f32_16x16x32_bf16 v[102:105], a[200:203], v[154:157], 0\n"
    "ds_read_b64_tr_b16 a[102:103], v65 offset:34816\n"
    "v_mfma_f32_16x16x32_bf16 v[118:121], a[204:207], v[154:157], 0\n"
    "ds_read_b64_tr_b16 a[96:97], v62 offset:32768\n"
    "buffer_store_dwordx4 v[46:49], v240, s[36:39], s83 offen sc1\n"
    "v_mfma_f32_16x16x32_bf16 v[22:25], a[192:195], v[154:157], v[22:25]\n"
    "ds_read_b64_tr_b16 a[98:99], v62 offset:34816\n"
    "s_waitcnt vmcnt(15)\n"
    "v_mfma_f32_16x16x32_bf16 v[98:101], a[212:215], v[158:161], v[98:101]\n"
    "ds_read_b64_tr_b16 a[40:41], v63 offset:49152\n"
    "v_mfma_f32_16x16x32_bf16 v[102:105], a[216:219], v[158:161], v[102:105]\n"
    "ds_read_b64_tr_b16 a[42:43], v63 offset:51200\n"
    "buffer_load_dwordx4 a[0:3], v247, s[36:39], s57 offen sc1\n"
    "v_mfma_f32_16x16x32_bf16 v[118:121], a[220:223], v[158:161], v[118:121]\n"
    "ds_read_b64_tr_b16 a[104:105], v64 offset:49152\n"
    "v_mfma_f32_16x16x32_bf16 v[22:25], a[208:211], v[158:161], v[22:25]\n"
    "ds_read_b64_tr_b16 a[106:107], v64 offset:51200\n"
    "s_waitcnt vmcnt(15)\n"
    "v_mfma_f32_16x16x32_bf16 v[98:101], a[228:231], v[162:165], v[98:101]\n"
    "ds_read_b64_tr_b16 a[168:169], v65 offset:49152\n"
    "v_mfma_f32_16x16x32_bf16 v[102:105], a[232:235], v[162:165], v[102:105]\n"
    "ds_read_b64_tr_b16 a[170:171], v65 offset:51200\n"
    "buffer_load_dwordx4 a[64:67], v247, s[36:39], s57 offen offset:1024 sc1\n"
    "v_mfma_f32_16x16x32_bf16 v[118:121], a[236:239], v[162:165], v[118:121]\n"
    "ds_read_b64_tr_b16 a[164:165], v62 offset:49152\n"
    "v_mfma_f32_16x16x32_bf16 v[22:25], a[224:227], v[162:165], v[22:25]\n"
    "ds_read_b64_tr_b16 a[166:167], v62 offset:51200\n"
    "v_xor_b32 v63, 32, v219\n"
    "s_waitcnt vmcnt(15)\n"
    "v_mfma_f32_16x16x32_bf16 v[98:101], a[244:247], v[166:169], v[98:101]\n"
    "v_xor_b32 v64, 64, v219\n"
    "v_xor_b32 v65, 0x60, v219\n"
    "v_mfma_f32_16x16x32_bf16 v[102:105], a[248:251], v[166:169], v[102:105]\n"
    "ds_read_b64_tr_b16 a[112:113], v63\n"
    "buffer_load_dwordx4 a[128:131], v247, s[36:39], s84 offen sc1\n"
    "v_mfma_f32_16x16x32_bf16 v[118:121], a[252:255], v[166:169], v[118:121]\n"
    "ds_read_b64_tr_b16 a[114:115], v65 offset:512\n"
    "v_mfma_f32_16x16x32_bf16 v[22:25], a[240:243], v[166:169], v[22:25]\n"
    "ds_read_b64_tr_b16 a[176:177], v64\n"
    "s_waitcnt vmcnt(15)\n"
    "v_mfma_f32_16x16x32_bf16 v[98:101], v[198:201], v[170:173], v[98:101]\n"
    "ds_read_b64_tr_b16 a[178:179], v219 offset:512\n"
    "v_mfma_f32_16x16x32_bf16 v[102:105], v[202:205], v[170:173], v[102:105]\n"
    "ds_read_b64_tr_b16 a[52:53], v65\n"
    "buffer_load_dwordx4 a[4:7], v247, s[36:39], s84 offen offset:1024 sc1\n"
    "v_mfma_f32_16x16x32_bf16 v[118:121], v[206:209], v[170:173], v[118:121]\n"
    "ds_read_b64_tr_b16 a[54:55], v63 offset:512\n"
    "v_mfma_f32_16x16x32_bf16 v[22:25], v[194:197], v[170:173], v[22:25]\n"
    "ds_read_b64_tr_b16 a[48:49], v219\n"
    "s_waitcnt vmcnt(15)\n"
    "v_mfma_f32_16x16x32_bf16 v[98:101], v[70:73], v[174:177], v[98:101]\n"
    "s_waitcnt vmcnt(8)\n"
    "ds_read_b128 a[92:95], v253\n"
    "v_mfma_f32_16x16x32_bf16 v[102:105], v[74:77], v[174:177], v[102:105]\n"
    "ds_read_b64_tr_b16 a[50:51], v64 offset:512\n"
    "buffer_load_dwordx4 a[68:71], v247, s[36:39], s91 offen sc1\n"
    "v_mfma_f32_16x16x32_bf16 v[118:121], v[78:81], v[174:177], v[118:121]\n"
    "ds_read_b64_tr_b16 a[180:181], v63 offset:16384\n"
    "v_mfma_f32_16x16x32_bf16 v[22:25], v[66:69], v[174:177], v[22:25]\n"
    "ds_read_b64_tr_b16 a[182:183], v65 offset:16896\n"
    "v_mfma_f32_16x16x32_bf16 v[98:101], a[160:163], v[178:181], v[98:101]\n"
    "ds_read_b128 a[156:159], v253 offset:1024\n"
    "v_mov_b32 v136, s4\n"
    "v_mfma_f32_16x16x32_bf16 v[102:105], a[36:39], v[178:181], v[102:105]\n"
    "ds_read_b64_tr_b16 a[56:57], v64 offset:16384\n"
    "buffer_load_dwordx4 a[132:135], v247, s[36:39], s91 offen offset:1024 sc1\n"
    "s_waitcnt lgkmcnt(6)\n"
    "v_mfma_f32_16x16x32_bf16 v[118:121], a[100:103], v[178:181], v[118:121]\n"
    "ds_read_b64_tr_b16 a[58:59], v219 offset:16896\n"
    "v_mfma_f32_16x16x32_bf16 v[98:101], a[40:43], v[182:185], v[98:101]\n"
    "ds_read_b128 a[32:35], v253 offset:2048\n"
    "v_mov_b32 v137, s5\n"
    "v_mfma_f32_16x16x32_bf16 v[102:105], a[104:107], v[182:185], v[102:105]\n"
    "ds_read_b64_tr_b16 a[120:121], v65 offset:16384\n"
    "v_mfma_f32_16x16x32_bf16 v[118:121], a[168:171], v[182:185], v[118:121]\n"
    "ds_read_b64_tr_b16 a[122:123], v63 offset:16896\n"
    "buffer_load_dwordx4 a[8:11], v247, s[36:39], s94 offen sc1\n"
    "s_waitcnt lgkmcnt(6)\n"
    "v_mfma_f32_16x16x32_bf16 v[98:101], a[112:115], a[92:95], v[98:101]\n"
    "ds_read_b64_tr_b16 a[116:117], v219 offset:16384\n"
    "s_waitcnt vmcnt(9)\n"
    "v_mfma_f32_16x16x32_bf16 v[102:105], a[176:179], a[92:95], v[102:105]\n"
    "ds_read_b64_tr_b16 a[118:119], v64 offset:16896\n"
    "v_mfma_f32_16x16x32_bf16 v[118:121], a[52:55], a[92:95], v[118:121]\n"
    "v_add_u32 v219, 0x10800, v219\n"
    "s_waitcnt lgkmcnt(6)\n"
    "v_mfma_f32_16x16x32_bf16 v[98:101], a[180:183], a[156:159], v[98:101]\n"
    "ds_read_b64_tr_b16 v[130:131], v238\n"
    "buffer_load_dwordx4 a[72:75], v247, s[36:39], s94 offen offset:1024 sc1\n"
    "s_waitcnt lgkmcnt(6)\n"
    "v_mfma_f32_16x16x32_bf16 v[102:105], a[56:59], a[156:159], v[102:105]\n"
    "v_add_u32 v63, 0x10800, v63\n"
    "v_add_u32 v64, 0x10800, v64\n"
    "v_add_u32 v65, 0x10800, v65\n"
    "s_waitcnt lgkmcnt(3)\n"
    "v_mfma_f32_16x16x32_bf16 v[118:121], a[120:123], a[156:159], v[118:121]\n"
    "ds_read_b64_tr_b16 a[60:61], v63\n"
    "ds_read_b64_tr_b16 a[62:63], v65 offset:512\n"
    "buffer_load_dwordx4 a[136:139], v247, s[36:39], s86 offen sc1\n"
    "ds_read_b64_tr_b16 a[124:125], v64\n"
    "ds_read_b64_tr_b16 a[126:127], v219 offset:512\n"
    "ds_read_b64_tr_b16 a[188:189], v65\n"
    "ds_read_b64_tr_b16 a[190:191], v63 offset:512\n"
    "ds_read_b64_tr_b16 a[184:185], v219\n"
    "ds_read_b64_tr_b16 a[186:187], v64 offset:512\n"
    "v_xor_b32 v238, 32, v238\n"
    "v_lshlrev_b32 v82, 16, v106\n"
    "v_and_b32 v83, s78, v106\n"
    "buffer_load_dwordx4 a[12:15], v247, s[36:39], s86 offen offset:1024 sc1\n"
    "v_lshlrev_b32 v84, 16, v107\n"
    "v_and_b32 v85, s78, v107\n"
    "s_waitcnt lgkmcnt(8)\n"
    "v_lshlrev_b32 v138, 16, v130\n"
    "v_and_b32 v139, s78, v130\n"
    "v_lshlrev_b32 v140, 16, v131\n"
    "v_and_b32 v141, s78, v131\n"
    "s_waitcnt lgkmcnt(6)\n"
    "v_mfma_f32_16x16x32_bf16 v[98:101], a[60:63], a[32:35], v[98:101]\n"
    "v_lshlrev_b32 v54, 16, v190\n"
    "v_and_b32 v55, s78, v190\n"
    "v_lshlrev_b32 v56, 16, v191\n"
    "v_and_b32 v57, s78, v191\n"
    "s_waitcnt lgkmcnt(4)\n"
    "v_mfma_f32_16x16x32_bf16 v[102:105], a[124:127], a[32:35], v[102:105]\n"
    "buffer_load_dwordx4 a[76:79], v247, s[36:39], s90 offen sc1\n"
    "v_add_u32 v247, 0x15800, v252\n"
    "s_waitcnt lgkmcnt(1)\n"
    "v_mfma_f32_16x16x32_bf16 v[118:121], a[188:191], a[32:35], v[118:121]\n"
    "ds_read_b64_tr_b16 v[132:133], v238\n"
    "v_xor_b32 v238, 0x60, v238\n"
    "v_mfma_f32_16x16x32_bf16 v[22:25], a[96:99], v[178:181], v[22:25]\n"
    "v_lshlrev_b32 v86, 16, v108\n"
    "v_and_b32 v87, s78, v108\n"
    "v_mfma_f32_16x16x32_bf16 v[22:25], a[164:167], v[182:185], v[22:25]\n"
    "v_cmp_u_f32 vcc, v98, v98\n"
    "v_lshlrev_b32 v88, 16, v109\n"
    "s_nop 0\n"
    "s_cbranch_vccnz .Lredo_14_%=\n"
    ".Lback_15_%=:\n"
    "buffer_load_dwordx4 a[140:143], v247, s[36:39], s57 offen sc1\n"
    "v_mfma_f32_16x16x32_bf16 v[22:25], a[48:51], a[92:95], v[22:25]\n"
    "ds_write_b128 v234, v[98:101]\n"
    "v_mfma_f32_16x16x32_bf16 v[22:25], a[116:119], a[156:159], v[22:25]\n"
    "v_and_b32 v89, s78, v109\n"
    "s_waitcnt lgkmcnt(2)\n"
    "v_mfma_f32_16x16x32_bf16 v[22:25], a[184:187], a[32:35], v[22:25]\n"
    "ds_write_b128 v233, v[102:105]\n"
    "buffer_load_dwordx4 a[16:19], v247, s[36:39], s57 offen offset:1024 sc1\n"
    "s_waitcnt lgkmcnt(2)\n"
    "v_lshlrev_b32 v142, 16, v132\n"
    "v_and_b32 v143, s78, v132\n"
    "v_lshlrev_b32 v144, 16, v133\n"
    "ds_write_b128 v231, v[118:121]\n"
    "s_waitcnt vmcnt(12)\n"
    "v_mfma_f32_16x16x32_bf16 v[98:101], a[196:199], a[0:3], 0\n"
    "v_and_b32 v145, s78, v133\n"
    "buffer_load_dwordx4 a[80:83], v247, s[36:39], s84 offen sc1\n"
    "v_mfma_f32_16x16x32_bf16 v[102:105], a[200:203], a[0:3], 0\n"
    "v_lshlrev_b32 v58, 16, v192\n"
    "v_and_b32 v59, s78, v192\n"
    "v_mfma_f32_16x16x32_bf16 v[118:121], a[204:207], a[0:3], 0\n"
    "v_lshlrev_b32 v60, 16, v193\n"
    "v_and_b32 v61, s78, v193\n"
    "s_waitcnt vmcnt(12)\n"
    "v_mfma_f32_16x16x32_bf16 v[98:101], a[212:215], a[64:67], v[98:101]\n"
    "ds_read_b64_tr_b16 v[130:131], v238\n"
    "v_xor_b32 v238, 32, v238\n"
    "v_mfma_f32_16x16x32_bf16 v[102:105], a[216:219], a[64:67], v[102:105]\n"
    "buffer_load_dwordx4 a[144:147], v247, s[36:39], s84 offen offset:1024 sc1\n"
    "v_mfma_f32_16x16x32_bf16 v[118:121], a[220:223], a[64:67], v[118:121]\n"
    "v_lshlrev_b32 v90, 16, v110\n"
    "v_and_b32 v91, s78, v110\n"
    "s_waitcnt vmcnt(12)\n"
    "v_mfma_f32_16x16x32_bf16 v[98:101], a[228:231], a[128:131], v[98:101]\n"
    "v_lshlrev_b32 v92, 16, v111\n"
    "v_and_b32 v93, s78, v111\n"
    "v_mfma_f32_16x16x32_bf16 v[102:105], a[232:235], a[128:131], v[102:105]\n"
    "v_mfma_f32_16x16x32_bf16 v[118:121], a[236:239], a[128:131], v[118:121]\n"
    "buffer_load_dwordx4 a[20:23], v247, s[36:39], s91 offen sc1\n"
    "s_waitcnt vmcnt(12)\n"
    "v_mfma_f32_16x16x32_bf16 v[98:101], a[244:247], a[4:7], v[98:101]\n"
    "v_mfma_f32_16x16x32_bf16 v[102:105], a[248:251], a[4:7], v[102:105]\n"
    "s_waitcnt lgkmcnt(0)\n"
    "s_barrier\n"
    "v_mfma_f32_16x16x32_bf16 v[118:121], a[252:255], a[4:7], v[118:121]\n"
    "ds_read_b128 v[14:17], v236\n"
    "v_lshlrev_b32 v146, 16, v130\n"
    "s_waitcnt vmcnt(11)\n"
    "v_mfma_f32_16x16x32_bf16 v[98:101], v[198:201], a[68:71], v[98:101]\n"
    "ds_read_b128 v[18:21], v236 offset:1024\n"
    "buffer_load_dwordx4 a[84:87], v247, s[36:39], s91 offen offset:1024 sc1\n"
    "v_mfma_f32_16x16x32_bf16 v[102:105], v[202:205], a[68:71], v[102:105]\n"
    "ds_read_b128 v[30:33], v236 offset:2048\n"
    "v_and_b32 v147, s78, v130\n"
    "v_mfma_f32_16x16x32_bf16 v[118:121], v[206:209], a[68:71], v[118:121]\n"
    "v_lshlrev_b32 v148, 16, v131\n"
    "v_and_b32 v149, s78, v131\n"
    "s_waitcnt vmcnt(11)\n"
    "v_mfma_f32_16x16x32_bf16 v[98:101], v[70:73], a[132:135], v[98:101]\n"
    "ds_read_b64_tr_b16 v[132:133], v238\n"
    "v_lshlrev_b32 v94, 16, v112\n"
    "v_mfma_f32_16x16x32_bf16 v[102:105], v[74:77], a[132:135], v[102:105]\n"
    "buffer_load_dwordx4 a[148:151], v247, s[36:39], s94 offen sc1\n"
    "v_mfma_f32_16x16x32_bf16 v[118:121], v[78:81], a[132:135], v[118:121]\n"
    "v_and_b32 v95, s78, v112\n"
    "v_lshlrev_b32 v96, 16, v113\n"
    "s_waitcnt vmcnt(11)\n"
    "v_mfma_f32_16x16x32_bf16 v[98:101], a[160:163], a[8:11], v[98:101]\n"
    "v_and_b32 v97, s78, v113\n"
    "v_mfma_f32_16x16x32_bf16 v[102:105], a[36:39], a[8:11], v[102:105]\n"
    "s_waitcnt lgkmcnt(2)\n"
    "v_add_f32 v14, v14, v18\n"
    "v_mfma_f32_16x16x32_bf16 v[118:121], a[100:103], a[8:11], v[118:121]\n"
    "buffer_load_dwordx4 a[24:27], v247, s[36:39], s94 offen offset:1024 sc1\n"
    "s_waitcnt vmcnt(11)\n"
    "v_mfma_f32_16x16x32_bf16 v[98:101], a[40:43], a[72:75], v[98:101]\n"
    "v_add_f32 v15, v15, v19\n"
    "v_add_f32 v16, v16, v20\n"
    "v_mfma_f32_16x16x32_bf16 v[102:105], a[104:107], a[72:75], v[102:105]\n"
    "v_add_f32 v17, v17, v21\n"
    "v_mfma_f32_16x16x32_bf16 v[118:121], a[168:171], a[72:75], v[118:121]\n"
    "s_waitcnt lgkmcnt(0)\n"
    "v_add_f32 v14, v14, v30\n"
    "s_waitcnt vmcnt(10)\n"
    "v_mfma_f32_16x16x32_bf16 v[98:101], a[112:115], a[136:139], v[98:101]\n"
    "buffer_load_dwordx4 a[88:91], v247, s[36:39], s86 offen sc1\n"
    "v_mfma_f32_16x16x32_bf16 v[102:105], a[176:179], a[136:139], v[102:105]\n"
    "v_add_f32 v15, v15, v31\n"
    "v_add_f32 v16, v16, v32\n"
    "v_mfma_f32_16x16x32_bf16 v[118:121], a[52:55], a[136:139], v[118:121]\n"
    "v_add_f32 v17, v17, v33\n"
    "v_add_f32 v22, v22, v14\n"
    "s_waitcnt vmcnt(10)\n"
    "v_mfma_f32_16x16x32_bf16 v[98:101], a[180:183], a[12:15], v[98:101]\n"
    "v_add_f32 v23, v23, v15\n"
    "v_add_f32 v24, v24, v16\n"
    "v_mfma_f32_16x16x32_bf16 v[102:105], a[56:59], a[12:15], v[102:105]\n"
    "buffer_load_dwordx4 a[152:155], v247, s[36:39], s86 offen offset:1024 sc1\n"
    "v_mfma_f32_16x16x32_bf16 v[118:121], a[120:123], a[12:15], v[118:121]\n"
    "v_add_f32 v25, v25, v17\n"
    "v_mul_f32 v122, s68, v22\n"
    "s_waitcnt vmcnt(10)\n"
    "v_mfma_f32_16x16x32_bf16 v[98:101], a[60:63], a[76:79], v[98:101]\n"
    "v_mul_f32 v123, s68, v23\n"
    "v_mul_f32 v124, s68, v24\n"
    "v_mfma_f32_16x16x32_bf16 v[102:105], a[124:127], a[76:79], v[102:105]\n"
    "v_mul_f32 v125, s68, v25\n"
    "v_mfma_f32_16x16x32_bf16 v[118:121], a[188:191], a[76:79], v[118:121]\n"
    "buffer_load_dwordx4 a[28:31], v247, s[36:39], s90 offen sc1\n"
    "v_add_u32 v247, 0x20400, v252\n"
    "v_mfma_f32_16x16x32_bf16 v[210:213], a[192:195], a[0:3], v[210:213]\n"
    "v_exp_f32 v122, v122\n"
    "v_mfma_f32_16x16x32_bf16 v[210:213], a[208:211], a[64:67], v[210:213]\n"
    "v_cmp_u_f32 vcc, v98, v98\n"
    "v_exp_f32 v123, v123\n"
    "s_nop 0\n"
    "s_cbranch_vccnz .Lredo_18_%=\n"
    ".Lback_19_%=:\n"
    "v_mfma_f32_16x16x32_bf16 v[210:213], a[224:227], a[128:131], v[210:213]\n"
    "ds_write_b128 v234, v[98:101] offset:12288\n"
    "buffer_load_dwordx4 v[154:157], v247, s[36:39], s57 offen sc1\n"
    "v_mfma_f32_16x16x32_bf16 v[210:213], a[240:243], a[4:7], v[210:213]\n"
    "v_exp_f32 v124, v124\n"
    "v_mfma_f32_16x16x32_bf16 v[210:213], v[194:197], a[68:71], v[210:213]\n"
    "ds_write_b128 v233, v[102:105] offset:12288\n"
    "v_mfma_f32_16x16x32_bf16 v[210:213], v[66:69], a[132:135], v[210:213]\n"
    "v_lshlrev_b32 v150, 16, v132\n"
    "buffer_load_dwordx4 v[158:161], v247, s[36:39], s57 offen offset:1024 sc1\n"
    "v_mfma_f32_16x16x32_bf16 v[210:213], a[96:99], a[8:11], v[210:213]\n"
    "ds_write_b128 v231, v[118:121] offset:12288\n"
    "v_mfma_f32_16x16x32_bf16 v[210:213], a[164:167], a[72:75], v[210:213]\n"
    "v_exp_f32 v125, v125\n"
    "v_mfma_f32_16x16x32_bf16 v[210:213], a[48:51], a[136:139], v[210:213]\n"
    "v_mul_f32 v22, v22, v138\n"
    "buffer_load_dwordx4 v[162:165], v247, s[36:39], s84 offen sc1\n"
    "v_mfma_f32_16x16x32_bf16 v[210:213], a[116:119], a[12:15], v[210:213]\n"
    "v_mul_f32 v23, v23, v139\n"
    "v_mul_f32 v24, v24, v140\n"
    "v_mfma_f32_16x16x32_bf16 v[210:213], a[184:187], a[76:79], v[210:213]\n"
    "v_mul_f32 v25, v25, v141\n"
    "v_add_f32 v122, 1.0, v122\n"
    "s_waitcnt vmcnt(13)\n"
    "v_mfma_f32_16x16x32_bf16 v[98:101], a[196:199], a[140:143], 0\n"
    "v_add_f32 v123, 1.0, v123\n"
    "v_add_f32 v124, 1.0, v124\n"
    "v_mfma_f32_16x16x32_bf16 v[102:105], a[200:203], a[140:143], 0\n"
    "buffer_load_dwordx4 v[166:169], v247, s[36:39], s84 offen offset:1024 sc1\n"
    "v_mfma_f32_16x16x32_bf16 v[118:121], a[204:207], a[140:143], 0\n"
    "v_add_f32 v125, 1.0, v125\n"
    "s_waitcnt vmcnt(13)\n"
    "v_mfma_f32_16x16x32_bf16 v[98:101], a[212:215], a[16:19], v[98:101]\n"
    "s_waitcnt lgkmcnt(0)\n"
    "v_mfma_f32_16x16x32_bf16 v[102:105], a[216:219], a[16:19], v[102:105]\n"
    "s_barrier\n"
    "ds_read_b128 v[38:41], v236 offset:12288\n"
    "v_mfma_f32_16x16x32_bf16 v[118:121], a[220:223], a[16:19], v[118:121]\n"
    "ds_read_b128 v[46:49], v236 offset:13312\n"
    "buffer_load_dwordx4 v[170:173], v247, s[36:39], s91 offen sc1\n"
    "s_waitcnt vmcnt(13)\n"
    "v_mfma_f32_16x16x32_bf16 v[98:101], a[228:231], a[80:83], v[98:101]\n"
    "ds_read_b128 v[50:53], v236 offset:14336\n"
    "v_mfma_f32_16x16x32_bf16 v[102:105], a[232:235], a[80:83], v[102:105]\n"
    "v_rcp_f32 v122, v122\n"
    "v_mfma_f32_16x16x32_bf16 v[118:121], a[236:239], a[80:83], v[118:121]\n"
    "v_rcp_f32 v123, v123\n"
    "s_waitcnt vmcnt(12)\n"
    "v_mfma_f32_16x16x32_bf16 v[98:101], a[244:247], a[144:147], v[98:101]\n"
    "buffer_load_dwordx4 v[174:177], v247, s[36:39], s91 offen offset:1024 sc1\n"
    "v_mfma_f32_16x16x32_bf16 v[102:105], a[248:251], a[144:147], v[102:105]\n"
    "v_rcp_f32 v124, v124\n"
    "v_mfma_f32_16x16x32_bf16 v[118:121], a[252:255], a[144:147], v[118:121]\n"
    "v_rcp_f32 v125, v125\n"
    "s_waitcnt vmcnt(12)\n"
    "v_mfma_f32_16x16x32_bf16 v[98:101], v[198:201], a[20:23], v[98:101]\n"
    "v_fmac_f32 v82, v22, v122\n"
    "v_fmac_f32 v83, v23, v123\n"
    "v_mfma_f32_16x16x32_bf16 v[102:105], v[202:205], a[20:23], v[102:105]\n"
    "buffer_load_dwordx4 v[178:181], v247, s[36:39], s94 offen sc1\n"
    "v_mfma_f32_16x16x32_bf16 v[118:121], v[206:209], a[20:23], v[118:121]\n"
    "v_fmac_f32 v84, v24, v124\n"
    "v_fmac_f32 v85, v25, v125\n"
    "s_waitcnt vmcnt(12)\n"
    "v_mfma_f32_16x16x32_bf16 v[98:101], v[70:73], a[84:87], v[98:101]\n"
    "v_cvt_pk_bf16_f32 v122, v82, v83\n"
    "v_cvt_pk_bf16_f32 v123, v84, v85\n"
    "v_mfma_f32_16x16x32_bf16 v[102:105], v[74:77], a[84:87], v[102:105]\n"
    "v_and_b32 v151, s78, v132\n"
    "v_lshlrev_b32 v152, 16, v133\n"
    "v_mfma_f32_16x16x32_bf16 v[118:121], v[78:81], a[84:87], v[118:121]\n"
    "buffer_load_dwordx4 v[182:185], v247, s[36:39], s94 offen offset:1024 sc1\n"
    "s_waitcnt vmcnt(12)\n"
    "v_mfma_f32_16x16x32_bf16 v[98:101], a[160:163], a[148:151], v[98:101]\n"
    "v_and_b32 v153, s78, v133\n"
    "v_mfma_f32_16x16x32_bf16 v[102:105], a[36:39], a[148:151], v[102:105]\n"
    "v_mfma_f32_16x16x32_bf16 v[118:121], a[100:103], a[148:151], v[118:121]\n"
    "s_waitcnt vmcnt(11)\n"
    "v_mfma_f32_16x16x32_bf16 v[98:101], a[40:43], a[24:27], v[98:101]\n"
    "buffer_load_dwordx4 a[92:95], v247, s[36:39], s86 offen sc1\n"
    "v_mfma_f32_16x16x32_bf16 v[102:105], a[104:107], a[24:27], v[102:105]\n"
    "v_mfma_f32_16x16x32_bf16 v[118:121], a[168:171], a[24:27], v[118:121]\n"
    "s_waitcnt vmcnt(11)\n"
    "v_mfma_f32_16x16x32_bf16 v[98:101], a[112:115], a[88:91], v[98:101]\n"
    "v_mfma_f32_16x16x32_bf16 v[102:105], a[176:179], a[88:91], v[102:105]\n"
    "buffer_load_dwordx4 a[156:159], v247, s[36:39], s86 offen offset:1024 sc1\n"
    "v_mfma_f32_16x16x32_bf16 v[118:121], a[52:55], a[88:91], v[118:121]\n"
    "s_waitcnt vmcnt(11)\n"
    "v_mfma_f32_16x16x32_bf16 v[98:101], a[180:183], a[152:155], v[98:101]\n"
    "v_mfma_f32_16x16x32_bf16 v[102:105], a[56:59], a[152:155], v[102:105]\n"
    "v_mfma_f32_16x16x32_bf16 v[118:121], a[120:123], a[152:155], v[118:121]\n"
    "buffer_load_dwordx4 a[32:35], v247, s[36:39], s90 offen sc1\n"
    "s_waitcnt vmcnt(11)\n"
    "v_mfma_f32_16x16x32_bf16 v[98:101], a[60:63], a[28:31], v[98:101]\n"
    "v_mfma_f32_16x16x32_bf16 v[102:105], a[124:127], a[28:31], v[102:105]\n"
    "v_mfma_f32_16x16x32_bf16 v[118:121], a[188:191], a[28:31], v[118:121]\n"
    "v_mfma_f32_16x16x32_bf16 v[54:57], a[192:195], a[140:143], v[54:57]\n"
    "v_mfma_f32_16x16x32_bf16 v[54:57], a[208:211], a[16:19], v[54:57]\n"
    "buffer_store_dwordx2 v[122:123], v237, s[40:43], s25 offen\n"
    "s_add_u32 s25, s25, 0x4000\n"
    "s_waitcnt lgkmcnt(0)\n"
    "v_add_f32 v38, v38, v46\n"
    "v_cmp_u_f32 vcc, v98, v98\n"
    "v_add_f32 v39, v39, v47\n"
    "v_add_f32 v40, v40, v48\n"
    "s_cbranch_vccnz .Lredo_22_%=\n"
    ".Lback_23_%=:\n"
    "v_mfma_f32_16x16x32_bf16 v[54:57], a[224:227], a[80:83], v[54:57]\n"
    "ds_write_b128 v234, v[98:101]\n"
    "v_mfma_f32_16x16x32_bf16 v[54:57], a[240:243], a[144:147], v[54:57]\n"
    "v_add_f32 v41, v41, v49\n"
    "v_add_f32 v38, v38, v50\n"
    "v_mfma_f32_16x16x32_bf16 v[54:57], v[194:197], a[20:23], v[54:57]\n"
    "buffer_store_dwordx2 v[136:137], v227, s[36:39], s18 offen sc1\n"
    "v_mfma_f32_16x16x32_bf16 v[54:57], v[66:69], a[84:87], v[54:57]\n"
    "ds_write_b128 v233, v[102:105]\n"
    "v_mfma_f32_16x16x32_bf16 v[54:57], a[96:99], a[148:151], v[54:57]\n"
    "v_add_f32 v39, v39, v51\n"
    "v_add_f32 v40, v40, v52\n"
    "v_mfma_f32_16x16x32_bf16 v[54:57], a[164:167], a[24:27], v[54:57]\n"
    "v_mfma_f32_16x16x32_bf16 v[54:57], a[48:51], a[88:91], v[54:57]\n"
    "ds_write_b128 v231, v[118:121]\n"
    "v_mfma_f32_16x16x32_bf16 v[54:57], a[116:119], a[152:155], v[54:57]\n"
    "v_add_f32 v41, v41, v53\n"
    "v_add_f32 v210, v210, v38\n"
    "v_mfma_f32_16x16x32_bf16 v[54:57], a[184:187], a[28:31], v[54:57]\n"
    "v_add_f32 v211, v211, v39\n"
    "v_add_f32 v212, v212, v40\n"
    "s_waitcnt vmcnt(12)\n"
    "v_mfma_f32_16x16x32_bf16 v[98:101], a[196:199], v[154:157], 0\n"
    "v_add_f32 v213, v213, v41\n"
    "v_mul_f32 v122, s68, v210\n"
    "v_mfma_f32_16x16x32_bf16 v[102:105], a[200:203], v[154:157], 0\n"
    "v_mul_f32 v123, s68, v211\n"
    "v_mul_f32 v124, s68, v212\n"
    "v_mfma_f32_16x16x32_bf16 v[118:121], a[204:207], v[154:157], 0\n"
    "v_mul_f32 v125, s68, v213\n"
    "s_waitcnt vmcnt(11)\n"
    "v_mfma_f32_16x16x32_bf16 v[98:101], a[212:215], v[158:161], v[98:101]\n"
    "v_exp_f32 v122, v122\n"
    "v_mfma_f32_16x16x32_bf16 v[102:105], a[216:219], v[158:161], v[102:105]\n"
    "v_exp_f32 v123, v123\n"
    "v_mfma_f32_16x16x32_bf16 v[118:121], a[220:223], v[158:161], v[118:121]\n"
    "v_exp_f32 v124, v124\n"
    "s_waitcnt vmcnt(10)\n"
    "v_mfma_f32_16x16x32_bf16 v[98:101], a[228:231], v[162:165], v[98:101]\n"
    "v_exp_f32 v125, v125\n"
    "v_mfma_f32_16x16x32_bf16 v[102:105], a[232:235], v[162:165], v[102:105]\n"
    "s_waitcnt lgkmcnt(0)\n"
    "s_barrier\n"
    "v_mfma_f32_16x16x32_bf16 v[118:121], a[236:239], v[162:165], v[118:121]\n"
    "ds_read_b128 v[14:17], v236\n"
    "v_mul_f32 v210, v210, v142\n"
    "s_waitcnt vmcnt(9)\n"
    "v_mfma_f32_16x16x32_bf16 v[98:101], a[244:247], v[166:169], v[98:101]\n"
    "ds_read_b128 v[18:21], v236 offset:1024\n"
    "v_mul_f32 v211, v211, v143\n"
    "v_mfma_f32_16x16x32_bf16 v[102:105], a[248:251], v[166:169], v[102:105]\n"
    "ds_read_b128 v[30:33], v236 offset:2048\n"
    "v_mul_f32 v212, v212, v144\n"
    "v_mfma_f32_16x16x32_bf16 v[118:121], a[252:255], v[166:169], v[118:121]\n"
    "v_mul_f32 v213, v213, v145\n"
    "v_add_f32 v122, 1.0, v122\n"
    "s_waitcnt vmcnt(8)\n"
    "v_mfma_f32_16x16x32_bf16 v[98:101], v[198:201], v[170:173], v[98:101]\n"
    "v_add_f32 v123, 1.0, v123\n"
    "v_add_f32 v124, 1.0, v124\n"
    "v_mfma_f32_16x16x32_bf16 v[102:105], v[202:205], v[170:173], v[102:105]\n"
    "v_add_f32 v125, 1.0, v125\n"
    "v_mfma_f32_16x16x32_bf16 v[118:121], v[206:209], v[170:173], v[118:121]\n"
    "v_rcp_f32 v122, v122\n"
    "s_waitcnt vmcnt(7)\n"
    "v_mfma_f32_16x16x32_bf16 v[98:101], v[70:73], v[174:177], v[98:101]\n"
    "v_rcp_f32 v123, v123\n"
    "v_mfma_f32_16x16x32_bf16 v[102:105], v[74:77], v[174:177], v[102:105]\n"
    "v_rcp_f32 v124, v124\n"
    "v_mfma_f32_16x16x32_bf16 v[118:121], v[78:81], v[174:177], v[118:121]\n"
    "v_rcp_f32 v125, v125\n"
    "s_waitcnt vmcnt(6)\n"
    "v_mfma_f32_16x16x32_bf16 v[98:101], a[160:163], v[178:181], v[98:101]\n"
    "v_fmac_f32 v86, v210, v122\n"
    "v_fmac_f32 v87, v211, v123\n"
    "v_mfma_f32_16x16x32_bf16 v[102:105], a[36:39], v[178:181], v[102:105]\n"
    "v_fmac_f32 v88, v212, v124\n"
    "v_fmac_f32 v89, v213, v125\n"
    "v_mfma_f32_16x16x32_bf16 v[118:121], a[100:103], v[178:181], v[118:121]\n"
    "v_cvt_pk_bf16_f32 v122, v86, v87\n"
    "v_cvt_pk_bf16_f32 v123, v88, v89\n"
    "s_waitcnt vmcnt(5)\n"
    "v_mfma_f32_16x16x32_bf16 v[98:101], a[40:43], v[182:185], v[98:101]\n"
    "buffer_store_dwordx2 v[122:123], v237, s[40:43], s25 offen\n"
    "v_mfma_f32_16x16x32_bf16 v[102:105], a[104:107], v[182:185], v[102:105]\n"
    "s_add_u32 s25, s25, 0x4000\n"
    "v_mfma_f32_16x16x32_bf16 v[118:121], a[168:171], v[182:185], v[118:121]\n"
    "s_waitcnt lgkmcnt(0)\n"
    "v_add_f32 v14, v14, v18\n"
    "s_waitcnt vmcnt(5)\n"
    "v_mfma_f32_16x16x32_bf16 v[98:101], a[112:115], a[92:95], v[98:101]\n"
    "v_add_f32 v15, v15, v19\n"
    "v_add_f32 v16, v16, v20\n"
    "v_mfma_f32_16x16x32_bf16 v[102:105], a[176:179], a[92:95], v[102:105]\n"
    "v_add_f32 v17, v17, v21\n"
    "v_add_f32 v14, v14, v30\n"
    "v_mfma_f32_16x16x32_bf16 v[118:121], a[52:55], a[92:95], v[118:121]\n"
    "v_add_f32 v15, v15, v31\n"
    "v_add_f32 v16, v16, v32\n"
    "s_waitcnt vmcnt(4)\n"
    "v_mfma_f32_16x16x32_bf16 v[98:101], a[180:183], a[156:159], v[98:101]\n"
    "v_add_f32 v17, v17, v33\n"
    "v_add_f32 v54, v54, v14\n"
    "v_mfma_f32_16x16x32_bf16 v[102:105], a[56:59], a[156:159], v[102:105]\n"
    "v_add_f32 v55, v55, v15\n"
    "v_add_f32 v56, v56, v16\n"
    "v_mfma_f32_16x16x32_bf16 v[118:121], a[120:123], a[156:159], v[118:121]\n"
    "v_add_f32 v57, v57, v17\n"
    "v_mul_f32 v122, s68, v54\n"
    "s_waitcnt vmcnt(3)\n"
    "v_mfma_f32_16x16x32_bf16 v[98:101], a[60:63], a[32:35], v[98:101]\n"
    "v_mul_f32 v123, s68, v55\n"
    "v_mul_f32 v124, s68, v56\n"
    "v_mfma_f32_16x16x32_bf16 v[102:105], a[124:127], a[32:35], v[102:105]\n"
    "v_mul_f32 v125, s68, v57\n"
    "v_mfma_f32_16x16x32_bf16 v[118:121], a[188:191], a[32:35], v[118:121]\n"
    "v_exp_f32 v122, v122\n"
    "v_mfma_f32_16x16x32_bf16 v[58:61], a[192:195], v[154:157], v[58:61]\n"
    "v_exp_f32 v123, v123\n"
    "v_mfma_f32_16x16x32_bf16 v[58:61], a[208:211], v[158:161], v[58:61]\n"
    "v_cmp_u_f32 vcc, v98, v98\n"
    "v_exp_f32 v124, v124\n"
    "s_nop 0\n"
    "s_cbranch_vccnz .Lredo_26_%=\n"
    ".Lback_27_%=:\n"
    "v_mfma_f32_16x16x32_bf16 v[58:61], a[224:227], v[162:165], v[58:61]\n"
    "ds_write_b128 v234, v[98:101] offset:12288\n"
    "v_mfma_f32_16x16x32_bf16 v[58:61], a[240:243], v[166:169], v[58:61]\n"
    "v_exp_f32 v125, v125\n"
    "v_mfma_f32_16x16x32_bf16 v[58:61], v[194:197], v[170:173], v[58:61]\n"
    "v_mfma_f32_16x16x32_bf16 v[58:61], v[66:69], v[174:177], v[58:61]\n"
    "ds_write_b128 v233, v[102:105] offset:12288\n"
    "v_mfma_f32_16x16x32_bf16 v[58:61], a[96:99], v[178:181], v[58:61]\n"
    "v_mul_f32 v54, v54, v146\n"
    "v_mul_f32 v55, v55, v147\n"
    "v_mfma_f32_16x16x32_bf16 v[58:61], a[164:167], v[182:185], v[58:61]\n"
    "v_mfma_f32_16x16x32_bf16 v[58:61], a[48:51], a[92:95], v[58:61]\n"
    "ds_write_b128 v231, v[118:121] offset:12288\n"
    "v_mfma_f32_16x16x32_bf16 v[58:61], a[116:119], a[156:159], v[58:61]\n"
    "v_mul_f32 v56, v56, v148\n"
    "v_mul_f32 v57, v57, v149\n"
    "v_mfma_f32_16x16x32_bf16 v[58:61], a[184:187], a[32:35], v[58:61]\n"
    "v_add_f32 v122, 1.0, v122\n"
    "v_add_f32 v123, 1.0, v123\n"
    "v_add_f32 v124, 1.0, v124\n"
    "v_add_f32 v125, 1.0, v125\n"
    "v_rcp_f32 v122, v122\n"
    "v_rcp_f32 v123, v123\n"
    "v_rcp_f32 v124, v124\n"
    "v_rcp_f32 v125, v125\n"
    "v_fmac_f32 v90, v54, v122\n"
    "v_fmac_f32 v91, v55, v123\n"
    "v_fmac_f32 v92, v56, v124\n"
    "v_fmac_f32 v93, v57, v125\n"
    "v_cvt_pk_bf16_f32 v122, v90, v91\n"
    "v_cvt_pk_bf16_f32 v123, v92, v93\n"
    "buffer_store_dwordx2 v[122:123], v237, s[40:43], s25 offen\n"
    "s_add_u32 s25, s25, 0x4000\n"
    "s_waitcnt lgkmcnt(0)\n"
    "s_barrier\n"
    "ds_read_b128 v[38:41], v236 offset:12288\n"
    "ds_read_b128 v[46:49], v236 offset:13312\n"
    "ds_read_b128 v[50:53], v236 offset:14336\n"
    "s_waitcnt lgkmcnt(1)\n"
    "v_pk_add_f32 v[38:39], v[38:39], v[46:47]\n"
    "v_pk_add_f32 v[40:41], v[40:41], v[48:49]\n"
    "s_waitcnt lgkmcnt(0)\n"
    "v_pk_add_f32 v[58:59], v[58:59], v[50:51]\n"
    "v_pk_add_f32 v[60:61], v[60:61], v[52:53]\n"
    "v_pk_add_f32 v[58:59], v[58:59], v[38:39]\n"
    "v_pk_add_f32 v[60:61], v[60:61], v[40:41]\n"
    "v_pk_mul_f32 v[122:123], v[58:59], s[68:69] op_sel_hi:[1,0]\n"
    "v_pk_mul_f32 v[124:125], v[60:61], s[68:69] op_sel_hi:[1,0]\n"
    "v_exp_f32 v122, v122\n"
    "v_exp_f32 v123, v123\n"
    "v_exp_f32 v124, v124\n"
    "v_exp_f32 v125, v125\n"
    "v_pk_mul_f32 v[58:59], v[58:59], v[150:151]\n"
    "v_pk_mul_f32 v[60:61], v[60:61], v[152:153]\n"
    "v_pk_add_f32 v[122:123], v[122:123], 1.0 op_sel_hi:[1,0]\n"
    "v_pk_add_f32 v[124:125], v[124:125], 1.0 op_sel_hi:[1,0]\n"
    "v_rcp_f32 v122, v122\n"
    "v_rcp_f32 v123, v123\n"
    "v_rcp_f32 v124, v124\n"
    "v_rcp_f32 v125, v125\n"
    "v_pk_fma_f32 v[94:95], v[58:59], v[122:123], v[94:95]\n"
    "v_pk_fma_f32 v[96:97], v[60:61], v[124:125], v[96:97]\n"
    "v_cvt_pk_bf16_f32 v122, v94, v95\n"
    "v_cvt_pk_bf16_f32 v123, v96, v97\n"
    "buffer_store_dwordx2 v[122:123], v237, s[40:43], s25 offen\n"
    "s_endpgm\n"
    ".Lspin_1_%=:\n"
    "s_mov_b32 s85, 20000\n"
    ".Lloop_3_%=:\n"
    "buffer_load_dwordx2 v[248:249], v229, s[36:39], s20 offen sc1\n"
    "s_waitcnt vmcnt(0)\n"
    "v_cmp_ne_u64 vcc, s[88:89], v[248:249]\n"
    "s_sub_u32 s85, s85, 1\n"
    "s_nop 1\n"
    "s_cbranch_vccz .Lback_2_%=\n"
    "s_cmp_lg_u32 s85, 0\n"
    "s_cbranch_scc1 .Lloop_3_%=\n"
    "s_branch .Lback_2_%=\n"
    ".Lspin_4_%=:\n"
    "s_mov_b32 s85, 20000\n"
    ".Lloop_6_%=:\n"
    "buffer_load_dwordx2 v[246:247], v228, s[36:39], s23 offen sc1\n"
    "s_waitcnt vmcnt(0)\n"
    "v_cmp_ne_u64 vcc, s[88:89], v[246:247]\n"
    "s_sub_u32 s85, s85, 1\n"
    "s_nop 1\n"
    "s_cbranch_vccz .Lback_5_%=\n"
    "s_cmp_lg_u32 s85, 0\n"
    "s_cbranch_scc1 .Lloop_6_%=\n"
    "s_branch .Lback_5_%=\n"
    ".Lspin_7_%=:\n"
    "s_mov_b32 s85, 20000\n"
    ".Lloop_9_%=:\n"
    "buffer_load_dwordx2 v[248:249], v228, s[36:39], s20 offen sc1\n"
    "s_waitcnt vmcnt(0)\n"
    "v_cmp_ne_u64 vcc, s[88:89], v[248:249]\n"
    "s_sub_u32 s85, s85, 1\n"
    "s_nop 1\n"
    "s_cbranch_vccz .Lback_8_%=\n"
    "s_cmp_lg_u32 s85, 0\n"
    "s_cbranch_scc1 .Lloop_9_%=\n"
    "s_branch .Lback_8_%=\n"
    ".Lspin_10_%=:\n"
    "s_mov_b32 s85, 20000\n"
    ".Lloop_12_%=:\n"
    "buffer_load_dwordx2 v[136:137], v229, s[36:39], s20 offen sc1\n"
    "s_waitcnt vmcnt(0)\n"
    "v_cmp_ne_u64 vcc, s[88:89], v[136:137]\n"
    "s_sub_u32 s85, s85, 1\n"
    "s_nop 1\n"
    "s_cbranch_vccz .Lback_11_%=\n"
    "s_cmp_lg_u32 s85, 0\n"
    "s_cbranch_scc1 .Lloop_12_%=\n"
    "s_branch .Lback_11_%=\n"
    ".Lredo_14_%=:\n"
    "s_mov_b32 s85, 20000\n"
    "s_nop 7\n"
    ".Lloop_16_%=:\n"
    "buffer_load_dwordx4 v[154:157], v252, s[36:39], s57 offen sc1\n"
    "buffer_load_dwordx4 v[158:161], v252, s[36:39], s57 offen offset:1024 sc1\n"
    "buffer_load_dwordx4 v[162:165], v252, s[36:39], s84 offen sc1\n"
    "buffer_load_dwordx4 v[166:169], v252, s[36:39], s84 offen offset:1024 sc1\n"
    "buffer_load_dwordx4 v[170:173], v252, s[36:39], s91 offen sc1\n"
    "buffer_load_dwordx4 v[174:177], v252, s[36:39], s91 offen offset:1024 sc1\n"
    "buffer_load_dwordx4 v[178:181], v252, s[36:39], s94 offen sc1\n"
    "buffer_load_dwordx4 v[182:185], v252, s[36:39], s94 offen offset:1024 sc1\n"
    "buffer_load_dwordx4 a[92:95], v252, s[36:39], s86 offen sc1\n"
    "buffer_load_dwordx4 a[156:159], v252, s[36:39], s86 offen offset:1024 sc1\n"
    "buffer_load_dwordx4 a[32:35], v252, s[36:39], s90 offen sc1\n"
    "s_waitcnt vmcnt(0)\n"
    "v_mfma_f32_16x16x32_bf16 v[98:101], a[196:199], v[154:157], 0\n"
    "v_mfma_f32_16x16x32_bf16 v[102:105], a[200:203], v[154:157], 0\n"
    "v_mfma_f32_16x16x32_bf16 v[118:121], a[204:207], v[154:157], 0\n"
    "v_mfma_f32_16x16x32_bf16 v[98:101], a[212:215], v[158:161], v[98:101]\n"
    "v_mfma_f32_16x16x32_bf16 v[102:105], a[216:219], v[158:161], v[102:105]\n"
    "v_mfma_f32_16x16x32_bf16 v[118:121], a[220:223], v[158:161], v[118:121]\n"
    "v_mfma_f32_16x16x32_bf16 v[98:101], a[228:231], v[162:165], v[98:101]\n"
    "v_mfma_f32_16x16x32_bf16 v[102:105], a[232:235], v[162:165], v[102:105]\n"
    "v_mfma_f32_16x16x32_bf16 v[118:121], a[236:239], v[162:165], v[118:121]\n"
    "v_mfma_f32_16x16x32_bf16 v[98:101], a[244:247], v[166:169], v[98:101]\n"
    "v_mfma_f32_16x16x32_bf16 v[102:105], a[248:251], v[166:169], v[102:105]\n"
    "v_mfma_f32_16x16x32_bf16 v[118:121], a[252:255], v[166:169], v[118:121]\n"
    "v_mfma_f32_16x16x32_bf16 v[98:101], v[198:201], v[170:173], v[98:101]\n"
    "v_mfma_f32_16x16x32_bf16 v[102:105], v[202:205], v[170:173], v[102:105]\n"
    "v_mfma_f32_16x16x32_bf16 v[118:121], v[206:209], v[170:173], v[118:121]\n"
    "v_mfma_f32_16x16x32_bf16 v[98:101], v[70:73], v[174:177], v[98:101]\n"
    "v_mfma_f32_16x16x32_bf16 v[102:105], v[74:77], v[174:177], v[102:105]\n"
    "v_mfma_f32_16x16x32_bf16 v[118:121], v[78:81], v[174:177], v[118:121]\n"
    "v_mfma_f32_16x16x32_bf16 v[98:101], a[160:163], v[178:181], v[98:101]\n"
    "v_mfma_f32_16x16x32_bf16 v[102:105], a[36:39], v[178:181], v[102:105]\n"
    "v_mfma_f32_16x16x32_bf16 v[118:121], a[100:103], v[178:181], v[118:121]\n"
    "v_mfma_f32_16x16x32_bf16 v[98:101], a[40:43], v[182:185], v[98:101]\n"
    "v_mfma_f32_16x16x32_bf16 v[102:105], a[104:107], v[182:185], v[102:105]\n"
    "v_mfma_f32_16x16x32_bf16 v[118:121], a[168:171], v[182:185], v[118:121]\n"
    "v_mfma_f32_16x16x32_bf16 v[98:101], a[112:115], a[92:95], v[98:101]\n"
    "v_mfma_f32_16x16x32_bf16 v[102:105], a[176:179], a[92:95], v[102:105]\n"
    "v_mfma_f32_16x16x32_bf16 v[118:121], a[52:55], a[92:95], v[118:121]\n"
    "v_mfma_f32_16x16x32_bf16 v[98:101], a[180:183], a[156:159], v[98:101]\n"
    "v_mfma_f32_16x16x32_bf16 v[102:105], a[56:59], a[156:159], v[102:105]\n"
    "v_mfma_f32_16x16x32_bf16 v[118:121], a[120:123], a[156:159], v[118:121]\n"
    "v_mfma_f32_16x16x32_bf16 v[98:101], a[60:63], a[32:35], v[98:101]\n"
    "v_mfma_f32_16x16x32_bf16 v[102:105], a[124:127], a[32:35], v[102:105]\n"
    "v_mfma_f32_16x16x32_bf16 v[118:121], a[188:191], a[32:35], v[118:121]\n"
    "s_nop 7\n"
    "s_nop 7\n"
    "v_cmp_u_f32 vcc, v98, v98\n"
    "s_sub_u32 s85, s85, 1\n"
    "s_nop 1\n"
    "s_cbranch_vccz .Ldone_17_%=\n"
    "s_cmp_lg_u32 s85, 0\n"
    "s_cbranch_scc1 .Lloop_16_%=\n"
    ".Ldone_17_%=:\n"
    "v_lshlrev_b32 v22, 16, v186\n"
    "v_and_b32 v23, s78, v186\n"
    "v_lshlrev_b32 v24, 16, v187\n"
    "v_and_b32 v25, s78, v187\n"
    "s_nop 1\n"
    "v_mfma_f32_16x16x32_bf16 v[22:25], a[192:195], v[154:157], v[22:25]\n"
    "v_mfma_f32_16x16x32_bf16 v[22:25], a[208:211], v[158:161], v[22:25]\n"
    "v_mfma_f32_16x16x32_bf16 v[22:25], a[224:227], v[162:165], v[22:25]\n"
    "v_mfma_f32_16x16x32_bf16 v[22:25], a[240:243], v[166:169], v[22:25]\n"
    "v_mfma_f32_16x16x32_bf16 v[22:25], v[194:197], v[170:173], v[22:25]\n"
    "v_mfma_f32_16x16x32_bf16 v[22:25], v[66:69], v[174:177], v[22:25]\n"
    "v_mfma_f32_16x16x32_bf16 v[22:25], a[96:99], v[178:181], v[22:25]\n"
    "v_mfma_f32_16x16x32_bf16 v[22:25], a[164:167], v[182:185], v[22:25]\n"
    "s_nop 7\n"
    "s_nop 7\n"
    "s_branch .Lback_15_%=\n"
    ".Lredo_18_%=:\n"
    "s_mov_b32 s85, 20000\n"
    "s_nop 7\n"
    ".Lloop_20_%=:\n"
    "v_add_u32 v242, 0xac00, v252\n"
    "buffer_load_dwordx4 a[0:3], v242, s[36:39], s57 offen sc1\n"
    "buffer_load_dwordx4 a[64:67], v242, s[36:39], s57 offen offset:1024 sc1\n"
    "buffer_load_dwordx4 a[128:131], v242, s[36:39], s84 offen sc1\n"
    "buffer_load_dwordx4 a[4:7], v242, s[36:39], s84 offen offset:1024 sc1\n"
    "buffer_load_dwordx4 a[68:71], v242, s[36:39], s91 offen sc1\n"
    "buffer_load_dwordx4 a[132:135], v242, s[36:39], s91 offen offset:1024 sc1\n"
    "buffer_load_dwordx4 a[8:11], v242, s[36:39], s94 offen sc1\n"
    "buffer_load_dwordx4 a[72:75], v242, s[36:39], s94 offen offset:1024 sc1\n"
    "buffer_load_dwordx4 a[136:139], v242, s[36:39], s86 offen sc1\n"
    "buffer_load_dwordx4 a[12:15], v242, s[36:39], s86 offen offset:1024 sc1\n"
    "buffer_load_dwordx4 a[76:79], v242, s[36:39], s90 offen sc1\n"
    "s_waitcnt vmcnt(0)\n"
    "v_mfma_f32_16x16x32_bf16 v[98:101], a[196:199], a[0:3], 0\n"
    "v_mfma_f32_16x16x32_bf16 v[102:105], a[200:203], a[0:3], 0\n"
    "v_mfma_f32_16x16x32_bf16 v[118:121], a[204:207], a[0:3], 0\n"
    "v_mfma_f32_16x16x32_bf16 v[98:101], a[212:215], a[64:67], v[98:101]\n"
    "v_mfma_f32_16x16x32_bf16 v[102:105], a[216:219], a[64:67], v[102:105]\n"
    "v_mfma_f32_16x16x32_bf16 v[118:121], a[220:223], a[64:67], v[118:121]\n"
    "v_mfma_f32_16x16x32_bf16 v[98:101], a[228:231], a[128:131], v[98:101]\n"
    "v_mfma_f32_16x16x32_bf16 v[102:105], a[232:235], a[128:131], v[102:105]\n"
    "v_mfma_f32_16x16x32_bf16 v[118:121], a[236:239], a[128:131], v[118:121]\n"
    "v_mfma_f32_16x16x32_bf16 v[98:101], a[244:247], a[4:7], v[98:101]\n"
    "v_mfma_f32_16x16x32_bf16 v[102:105], a[248:251], a[4:7], v[102:105]\n"
    "v_mfma_f32_16x16x32_bf16 v[118:121], a[252:255], a[4:7], v[118:121]\n"
    "v_mfma_f32_16x16x32_bf16 v[98:101], v[198:201], a[68:71], v[98:101]\n"
    "v_mfma_f32_16x16x32_bf16 v[102:105], v[202:205], a[68:71], v[102:105]\n"
    "v_mfma_f32_16x16x32_bf16 v[118:121], v[206:209], a[68:71], v[118:121]\n"
    "v_mfma_f32_16x16x32_bf16 v[98:101], v[70:73], a[132:135], v[98:101]\n"
    "v_mfma_f32_16x16x32_bf16 v[102:105], v[74:77], a[132:135], v[102:105]\n"
    "v_mfma_f32_16x16x32_bf16 v[118:121], v[78:81], a[132:135], v[118:121]\n"
    "v_mfma_f32_16x16x32_bf16 v[98:101], a[160:163], a[8:11], v[98:101]\n"
    "v_mfma_f32_16x16x32_bf16 v[102:105], a[36:39], a[8:11], v[102:105]\n"
    "v_mfma_f32_16x16x32_bf16 v[118:121], a[100:103], a[8:11], v[118:121]\n"
    "v_mfma_f32_16x16x32_bf16 v[98:101], a[40:43], a[72:75], v[98:101]\n"
    "v_mfma_f32_16x16x32_bf16 v[102:105], a[104:107], a[72:75], v[102:105]\n"
    "v_mfma_f32_16x16x32_bf16 v[118:121], a[168:171], a[72:75], v[118:121]\n"
    "v_mfma_f32_16x16x32_bf16 v[98:101], a[112:115], a[136:139], v[98:101]\n"
    "v_mfma_f32_16x16x32_bf16 v[102:105], a[176:179], a[136:139], v[102:105]\n"
    "v_mfma_f32_16x16x32_bf16 v[118:121], a[52:55], a[136:139], v[118:121]\n"
    "v_mfma_f32_16x16x32_bf16 v[98:101], a[180:183], a[12:15], v[98:101]\n"
    "v_mfma_f32_16x16x32_bf16 v[102:105], a[56:59], a[12:15], v[102:105]\n"
    "v_mfma_f32_16x16x32_bf16 v[118:121], a[120:123], a[12:15], v[118:121]\n"
    "v_mfma_f32_16x16x32_bf16 v[98:101], a[60:63], a[76:79], v[98:101]\n"
    "v_mfma_f32_16x16x32_bf16 v[102:105], a[124:127], a[76:79], v[102:105]\n"
    "v_mfma_f32_16x16x32_bf16 v[118:121], a[188:191], a[76:79], v[118:121]\n"
    "s_nop 7\n"
    "s_nop 7\n"
    "v_cmp_u_f32 vcc, v98, v98\n"
    "s_sub_u32 s85, s85, 1\n"
    "s_nop 1\n"
    "s_cbranch_vccz .Ldone_21_%=\n"
    "s_cmp_lg_u32 s85, 0\n"
    "s_cbranch_scc1 .Lloop_20_%=\n"
    ".Ldone_21_%=:\n"
    "v_lshlrev_b32 v210, 16, v188\n"
    "v_and_b32 v211, s78, v188\n"
    "v_lshlrev_b32 v212, 16, v189\n"
    "v_and_b32 v213, s78, v189\n"
    "s_nop 1\n"
    "v_mfma_f32_16x16x32_bf16 v[210:213], a[192:195], a[0:3], v[210:213]\n"
    "v_mfma_f32_16x16x32_bf16 v[210:213], a[208:211], a[64:67], v[210:213]\n"
    "s_nop 7\n"
    "s_nop 7\n"
    "s_branch .Lback_19_%=\n"
    ".Lredo_22_%=:\n"
    "s_mov_b32 s85, 20000\n"
    "s_nop 7\n"
    ".Lloop_24_%=:\n"
    "v_add_u32 v242, 0x15800, v252\n"
    "buffer_load_dwordx4 a[140:143], v242, s[36:39], s57 offen sc1\n"
    "buffer_load_dwordx4 a[16:19], v242, s[36:39], s57 offen offset:1024 sc1\n"
    "buffer_load_dwordx4 a[80:83], v242, s[36:39], s84 offen sc1\n"
    "buffer_load_dwordx4 a[144:147], v242, s[36:39], s84 offen offset:1024 sc1\n"
    "buffer_load_dwordx4 a[20:23], v242, s[36:39], s91 offen sc1\n"
    "buffer_load_dwordx4 a[84:87], v242, s[36:39], s91 offen offset:1024 sc1\n"
    "buffer_load_dwordx4 a[148:151], v242, s[36:39], s94 offen sc1\n"
    "buffer_load_dwordx4 a[24:27], v242, s[36:39], s94 offen offset:1024 sc1\n"
    "buffer_load_dwordx4 a[88:91], v242, s[36:39], s86 offen sc1\n"
    "buffer_load_dwordx4 a[152:155], v242, s[36:39], s86 offen offset:1024 sc1\n"
    "buffer_load_dwordx4 a[28:31], v242, s[36:39], s90 offen sc1\n"
    "s_waitcnt vmcnt(0)\n"
    "v_mfma_f32_16x16x32_bf16 v[98:101], a[196:199], a[140:143], 0\n"
    "v_mfma_f32_16x16x32_bf16 v[102:105], a[200:203], a[140:143], 0\n"
    "v_mfma_f32_16x16x32_bf16 v[118:121], a[204:207], a[140:143], 0\n"
    "v_mfma_f32_16x16x32_bf16 v[98:101], a[212:215], a[16:19], v[98:101]\n"
    "v_mfma_f32_16x16x32_bf16 v[102:105], a[216:219], a[16:19], v[102:105]\n"
    "v_mfma_f32_16x16x32_bf16 v[118:121], a[220:223], a[16:19], v[118:121]\n"
    "v_mfma_f32_16x16x32_bf16 v[98:101], a[228:231], a[80:83], v[98:101]\n"
    "v_mfma_f32_16x16x32_bf16 v[102:105], a[232:235], a[80:83], v[102:105]\n"
    "v_mfma_f32_16x16x32_bf16 v[118:121], a[236:239], a[80:83], v[118:121]\n"
    "v_mfma_f32_16x16x32_bf16 v[98:101], a[244:247], a[144:147], v[98:101]\n"
    "v_mfma_f32_16x16x32_bf16 v[102:105], a[248:251], a[144:147], v[102:105]\n"
    "v_mfma_f32_16x16x32_bf16 v[118:121], a[252:255], a[144:147], v[118:121]\n"
    "v_mfma_f32_16x16x32_bf16 v[98:101], v[198:201], a[20:23], v[98:101]\n"
    "v_mfma_f32_16x16x32_bf16 v[102:105], v[202:205], a[20:23], v[102:105]\n"
    "v_mfma_f32_16x16x32_bf16 v[118:121], v[206:209], a[20:23], v[118:121]\n"
    "v_mfma_f32_16x16x32_bf16 v[98:101], v[70:73], a[84:87], v[98:101]\n"
    "v_mfma_f32_16x16x32_bf16 v[102:105], v[74:77], a[84:87], v[102:105]\n"
    "v_mfma_f32_16x16x32_bf16 v[118:121], v[78:81], a[84:87], v[118:121]\n"
    "v_mfma_f32_16x16x32_bf16 v[98:101], a[160:163], a[148:151], v[98:101]\n"
    "v_mfma_f32_16x16x32_bf16 v[102:105], a[36:39], a[148:151], v[102:105]\n"
    "v_mfma_f32_16x16x32_bf16 v[118:121], a[100:103], a[148:151], v[118:121]\n"
    "v_mfma_f32_16x16x32_bf16 v[98:101], a[40:43], a[24:27], v[98:101]\n"
    "v_mfma_f32_16x16x32_bf16 v[102:105], a[104:107], a[24:27], v[102:105]\n"
    "v_mfma_f32_16x16x32_bf16 v[118:121], a[168:171], a[24:27], v[118:121]\n"
    "v_mfma_f32_16x16x32_bf16 v[98:101], a[112:115], a[88:91], v[98:101]\n"
    "v_mfma_f32_16x16x32_bf16 v[102:105], a[176:179], a[88:91], v[102:105]\n"
    "v_mfma_f32_16x16x32_bf16 v[118:121], a[52:55], a[88:91], v[118:121]\n"
    "v_mfma_f32_16x16x32_bf16 v[98:101], a[180:183], a[152:155], v[98:101]\n"
    "v_mfma_f32_16x16x32_bf16 v[102:105], a[56:59], a[152:155], v[102:105]\n"
    "v_mfma_f32_16x16x32_bf16 v[118:121], a[120:123], a[152:155], v[118:121]\n"
    "v_mfma_f32_16x16x32_bf16 v[98:101], a[60:63], a[28:31], v[98:101]\n"
    "v_mfma_f32_16x16x32_bf16 v[102:105], a[124:127], a[28:31], v[102:105]\n"
    "v_mfma_f32_16x16x32_bf16 v[118:121], a[188:191], a[28:31], v[118:121]\n"
    "s_nop 7\n"
    "s_nop 7\n"
    "v_cmp_u_f32 vcc, v98, v98\n"
    "s_sub_u32 s85, s85, 1\n"
    "s_nop 1\n"
    "s_cbranch_vccz .Ldone_25_%=\n"
    "s_cmp_lg_u32 s85, 0\n"
    "s_cbranch_scc1 .Lloop_24_%=\n"
    ".Ldone_25_%=:\n"
    "v_lshlrev_b32 v54, 16, v190\n"
    "v_and_b32 v55, s78, v190\n"
    "v_lshlrev_b32 v56, 16, v191\n"
    "v_and_b32 v57, s78, v191\n"
    "s_nop 1\n"
    "v_mfma_f32_16x16x32_bf16 v[54:57], a[192:195], a[140:143], v[54:57]\n"
    "v_mfma_f32_16x16x32_bf16 v[54:57], a[208:211], a[16:19], v[54:57]\n"
    "s_nop 7\n"
    "s_nop 7\n"
    "s_branch .Lback_23_%=\n"
    ".Lredo_26_%=:\n"
    "s_mov_b32 s85, 20000\n"
    "s_nop 7\n"
    ".Lloop_28_%=:\n"
    "v_add_u32 v242, 0x20400, v252\n"
    "buffer_load_dwordx4 v[154:157], v242, s[36:39], s57 offen sc1\n"
    "buffer_load_dwordx4 v[158:161], v242, s[36:39], s57 offen offset:1024 sc1\n"
    "buffer_load_dwordx4 v[162:165], v242, s[36:39], s84 offen sc1\n"
    "buffer_load_dwordx4 v[166:169], v242, s[36:39], s84 offen offset:1024 sc1\n"
    "buffer_load_dwordx4 v[170:173], v242, s[36:39], s91 offen sc1\n"
    "buffer_load_dwordx4 v[174:177], v242, s[36:39], s91 offen offset:1024 sc1\n"
    "buffer_load_dwordx4 v[178:181], v242, s[36:39], s94 offen sc1\n"
    "buffer_load_dwordx4 v[182:185], v242, s[36:39], s94 offen offset:1024 sc1\n"
    "buffer_load_dwordx4 a[92:95], v242, s[36:39], s86 offen sc1\n"
    "buffer_load_dwordx4 a[156:159], v242, s[36:39], s86 offen offset:1024 sc1\n"
    "buffer_load_dwordx4 a[32:35], v242, s[36:39], s90 offen sc1\n"
    "s_waitcnt vmcnt(0)\n"
    "v_mfma_f32_16x16x32_bf16 v[98:101], a[196:199], v[154:157], 0\n"
    "v_mfma_f32_16x16x32_bf16 v[102:105], a[200:203], v[154:157], 0\n"
    "v_mfma_f32_16x16x32_bf16 v[118:121], a[204:207], v[154:157], 0\n"
    "v_mfma_f32_16x16x32_bf16 v[98:101], a[212:215], v[158:161], v[98:101]\n"
    "v_mfma_f32_16x16x32_bf16 v[102:105], a[216:219], v[158:161], v[102:105]\n"
    "v_mfma_f32_16x16x32_bf16 v[118:121], a[220:223], v[158:161], v[118:121]\n"
    "v_mfma_f32_16x16x32_bf16 v[98:101], a[228:231], v[162:165], v[98:101]\n"
    "v_mfma_f32_16x16x32_bf16 v[102:105], a[232:235], v[162:165], v[102:105]\n"
    "v_mfma_f32_16x16x32_bf16 v[118:121], a[236:239], v[162:165], v[118:121]\n"
    "v_mfma_f32_16x16x32_bf16 v[98:101], a[244:247], v[166:169], v[98:101]\n"
    "v_mfma_f32_16x16x32_bf16 v[102:105], a[248:251], v[166:169], v[102:105]\n"
    "v_mfma_f32_16x16x32_bf16 v[118:121], a[252:255], v[166:169], v[118:121]\n"
    "v_mfma_f32_16x16x32_bf16 v[98:101], v[198:201], v[170:173], v[98:101]\n"
    "v_mfma_f32_16x16x32_bf16 v[102:105], v[202:205], v[170:173], v[102:105]\n"
    "v_mfma_f32_16x16x32_bf16 v[118:121], v[206:209], v[170:173], v[118:121]\n"
    "v_mfma_f32_16x16x32_bf16 v[98:101], v[70:73], v[174:177], v[98:101]\n"
    "v_mfma_f32_16x16x32_bf16 v[102:105], v[74:77], v[174:177], v[102:105]\n"
    "v_mfma_f32_16x16x32_bf16 v[118:121], v[78:81], v[174:177], v[118:121]\n"
    "v_mfma_f32_16x16x32_bf16 v[98:101], a[160:163], v[178:181], v[98:101]\n"
    "v_mfma_f32_16x16x32_bf16 v[102:105], a[36:39], v[178:181], v[102:105]\n"
    "v_mfma_f32_16x16x32_bf16 v[118:121], a[100:103], v[178:181], v[118:121]\n"
    "v_mfma_f32_16x16x32_bf16 v[98:101], a[40:43], v[182:185], v[98:101]\n"
    "v_mfma_f32_16x16x32_bf16 v[102:105], a[104:107], v[182:185], v[102:105]\n"
    "v_mfma_f32_16x16x32_bf16 v[118:121], a[168:171], v[182:185], v[118:121]\n"
    "v_mfma_f32_16x16x32_bf16 v[98:101], a[112:115], a[92:95], v[98:101]\n"
    "v_mfma_f32_16x16x32_bf16 v[102:105], a[176:179], a[92:95], v[102:105]\n"
    "v_mfma_f32_16x16x32_bf16 v[118:121], a[52:55], a[92:95], v[118:121]\n"
    "v_mfma_f32_16x16x32_bf16 v[98:101], a[180:183], a[156:159], v[98:101]\n"
    "v_mfma_f32_16x16x32_bf16 v[102:105], a[56:59], a[156:159], v[102:105]\n"
    "v_mfma_f32_16x16x32_bf16 v[118:121], a[120:123], a[156:159], v[118:121]\n"
    "v_mfma_f32_16x16x32_bf16 v[98:101], a[60:63], a[32:35], v[98:101]\n"
    "v_mfma_f32_16x16x32_bf16 v[102:105], a[124:127], a[32:35], v[102:105]\n"
    "v_mfma_f32_16x16x32_bf16 v[118:121], a[188:191], a[32:35], v[118:121]\n"
    "s_nop 7\n"
    "s_nop 7\n"
    "v_cmp_u_f32 vcc, v98, v98\n"
    "s_sub_u32 s85, s85, 1\n"
    "s_nop 1\n"
    "s_cbranch_vccz .Ldone_29_%=\n"
    "s_cmp_lg_u32 s85, 0\n"
    "s_cbranch_scc1 .Lloop_28_%=\n"
    ".Ldone_29_%=:\n"
    "v_lshlrev_b32 v58, 16, v192\n"
    "v_and_b32 v59, s78, v192\n"
    "v_lshlrev_b32 v60, 16, v193\n"
    "v_and_b32 v61, s78, v193\n"
    "s_nop 1\n"
    "v_mfma_f32_16x16x32_bf16 v[58:61], a[192:195], v[154:157], v[58:61]\n"
    "v_mfma_f32_16x16x32_bf16 v[58:61], a[208:211], v[158:161], v[58:61]\n"
    "s_nop 7\n"
    "s_nop 7\n"
    "s_branch .Lback_27_%=\n"
    ".Lend_%=:\n"
    :
    : "s"(__builtin_amdgcn_kernarg_segment_ptr()), "s"(blockIdx.x), "v"(threadIdx.x)
    : "v1", "v2", "v3", "v4", "v5", "v6", "v7", "v8", "v9", "v10", "v11", "v12", "v13", "v14", "v15", "v16", "v17", "v18", "v19", "v20", "v21", "v22", "v23", "v24", "v25", "v26", "v27", "v28", "v29", "v30", "v31", "v32", "v33", "v34", "v35", "v36", "v37", "v38", "v39", "v40", "v41", "v42", "v43", "v44", "v45", "v46", "v47", "v48", "v49", "v50", "v51", "v52", "v53", "v54", "v55", "v56", "v57", "v58", "v59", "v60", "v61", "v62", "v63", "v64", "v65", "v66", "v67", "v68", "v69", "v70", "v71", "v72", "v73", "v74", "v75", "v76", "v77", "v78", "v79", "v80", "v81", "v82", "v83", "v84", "v85", "v86", "v87", "v88", "v89", "v90", "v91", "v92", "v93", "v94", "v95", "v96", "v97", "v98", "v99", "v100", "v101", "v102", "v103", "v104", "v105", "v106", "v107", "v108", "v109", "v110", "v111", "v112", "v113", "v114", "v115", "v116", "v117", "v118", "v119", "v120", "v121", "v122", "v123", "v124", "v125", "v126", "v127", "v128", "v129", "v130", "v131", "v132", "v133", "v134", "v135", "v136", "v137", "v138", "v139", "v140", "v141", "v142", "v143", "v144", "v145", "v146", "v147", "v148", "v149", "v150", "v151", "v152", "v153", "v154", "v155", "v156", "v157", "v158", "v159", "v160", "v161", "v162", "v163", "v164", "v165", "v166", "v167", "v168", "v169", "v170", "v171", "v172", "v173", "v174", "v175", "v176", "v177", "v178", "v179", "v180", "v181", "v182", "v183", "v184", "v185", "v186", "v187", "v188", "v189", "v190", "v191", "v192", "v193", "v194", "v195", "v196", "v197", "v198", "v199", "v200", "v201", "v202", "v203", "v204", "v205", "v206", "v207", "v208", "v209", "v210", "v211", "v212", "v213", "v214", "v215", "v216", "v217", "v218", "v219", "v220", "v221", "v222", "v223", "v224", "v225", "v226", "v227", "v228", "v229", "v230", "v231", "v232", "v233", "v234", "v235", "v236", "v237", "v238", "v239", "v240", "v241", "v242", "v243", "v244", "v245", "v246", "v247", "v248", "v249", "v250", "v251", "v252", "v253", "v254", "v255", "a0", "a1", "a2", "a3", "a4", "a5", "a6", "a7", "a8", "a9", "a10", "a11", "a12", "a13", "a14", "a15", "a16", "a17", "a18", "a19", "a20", "a21", "a22", "a23", "a24", "a25", "a26", "a27", "a28", "a29", "a30", "a31", "a32", "a33", "a34", "a35", "a36", "a37", "a38", "a39", "a40", "a41", "a42", "a43", "a44", "a45", "a46", "a47", "a48", "a49", "a50", "a51", "a52", "a53", "a54", "a55", "a56", "a57", "a58", "a59", "a60", "a61", "a62", "a63", "a64", "a65", "a66", "a67", "a68", "a69", "a70", "a71", "a72", "a73", "a74", "a75", "a76", "a77", "a78", "a79", "a80", "a81", "a82", "a83", "a84", "a85", "a86", "a87", "a88", "a89", "a90", "a91", "a92", "a93", "a94", "a95", "a96", "a97", "a98", "a99", "a100", "a101", "a102", "a103", "a104", "a105", "a106", "a107", "a108", "a109", "a110", "a111", "a112", "a113", "a114", "a115", "a116", "a117", "a118", "a119", "a120", "a121", "a122", "a123", "a124", "a125", "a126", "a127", "a128", "a129", "a130", "a131", "a132", "a133", "a134", "a135", "a136", "a137", "a138", "a139", "a140", "a141", "a142", "a143", "a144", "a145", "a146", "a147", "a148", "a149", "a150", "a151", "a152", "a153", "a154", "a155", "a156", "a157", "a158", "a159", "a160", "a161", "a162", "a163", "a164", "a165", "a166", "a167", "a168", "a169", "a170", "a171", "a172", "a173", "a174", "a175", "a176", "a177", "a178", "a179", "a180", "a181", "a182", "a183", "a184", "a185", "a186", "a187", "a188", "a189", "a190", "a191", "a192", "a193", "a194", "a195", "a196", "a197", "a198", "a199", "a200", "a201", "a202", "a203", "a204", "a205", "a206", "a207", "a208", "a209", "a210", "a211", "a212", "a213", "a214", "a215", "a216", "a217", "a218", "a219", "a220", "a221", "a222", "a223", "a224", "a225", "a226", "a227", "a228", "a229", "a230", "a231", "a232", "a233", "a234", "a235", "a236", "a237", "a238", "a239", "a240", "a241", "a242", "a243", "a244", "a245", "a246", "a247", "a248", "a249", "a250", "a251", "a252", "a253", "a254", "a255", "s3", "s4", "s5", "s6", "s7", "s8", "s9", "s10", "s11", "s12", "s13", "s14", "s15", "s16", "s17", "s18", "s19", "s20", "s21", "s22", "s23", "s24", "s25", "s26", "s27", "s28", "s29", "s30", "s31", "s32", "s33", "s34", "s35", "s36", "s37", "s38", "s39", "s40", "s41", "s42", "s43", "s44", "s45", "s46", "s47", "s48", "s49", "s50", "s51", "s52", "s53", "s54", "s55", "s56", "s57", "s58", "s59", "s60", "s61", "s62", "s63", "s64", "s65", "s66", "s67", "s68", "s69", "s70", "s71", "s72", "s73", "s74", "s75", "s76", "s77", "s78", "s79", "s80", "s81", "s82", "s83", "s84", "s85", "s86", "s87", "s88", "s89", "s90", "s91", "s92", "s93", "s94", "s95", "s96", "s97", "s98", "s99", "vcc", "scc", "memory");
}

extern "C" void kernel_launch(void* const* d_in, const int* in_sizes, int n_in,
                              void* d_out, int out_size, void* d_ws, size_t ws_size,
                              hipStream_t stream) {
  Args a;
  const int order[7] = {0, 10, 11, 12, 13, -1, -2};
  for (int i = 0; i < 7; ++i) {
    const int k = order[i];
    a.d[i].p = k >= 0 ? d_in[k] : (k == -1 ? (const void*)((const char*)d_ws + 0) : (const void*)d_out);
    a.d[i].bytes = k >= 0 ? (unsigned)in_sizes[k] * 2u : (k == -1 ? 15073280u : (unsigned)out_size * 2u);
    a.d[i].word3 = 0x00020000u;
  }
  for (int i = 0; i < 8; ++i) a.par[i] = d_in[1 + i];
  a.fill[0] = a.fill[1] = 0;
  (void)hipFuncSetAttribute((const void*)fused_s4_mixer_kernel, hipFuncAttributeMaxDynamicSharedMemorySize, 163840);
  hipLaunchKernelGGL(fused_s4_mixer_kernel, dim3(256), dim3(256), 163840, stream, a);
}
```
